```python
import torch

def _softmax(x, axis=-1):
    return torch.softmax(x, dim=axis)

def _gelu(x):
    return torch.nn.functional.gelu(x, approximate='tanh')

def _silu(x):
    return torch.nn.functional.silu(x)

def _sigmoid(x):
    return torch.sigmoid(x)

def _layernorm(x, axis=-1, eps=1e-05):
    m = x.mean(dim=axis, keepdim=True)
    v = x.var(dim=axis, keepdim=True, unbiased=False)
    return (x - m) / torch.sqrt(v + eps)

def _scatter_add(tgt, idx, src, axis=0):
    return tgt.clone().index_add_(axis, idx, src)

def _segment_sum(src, seg, n):
    out = torch.zeros((n,) + src.shape[1:], dtype=src.dtype)
    return out.index_add_(0, seg, src)

def _pad(x, pw):
    flat = tuple((v for pair in reversed(pw) for v in pair))
    return torch.nn.functional.pad(x, flat)
kernel_name = 'adaLN_cond__sliding_block_cached__geglu_ffn__decode'

def setup_inputs():

    _w = lambda a: (lambda t: t.to(torch.bfloat16) if t.is_floating_point() else t)(torch.as_tensor(a))

    def _A_setup():
        B, Sq, D = (1, 1, 512)
        Hq, Hkv, Sk, Dh = (32, 8, 65536, 128)
        assert Hq % Hkv == 0, (Hq, Hkv)
        return {'x': _w((lambda *s: torch.randn(*s, dtype=torch.bfloat16) * 0.1)(B, Sq, D)), 'k_cache': _w((lambda *s: torch.randn(*s, dtype=torch.bfloat16) * 0.8)(B, Hkv, Sk, Dh)), 'v_cache': _w((lambda *s: torch.randn(*s, dtype=torch.bfloat16) * 1.6)(B, Hkv, Sk, Dh)), 'wq': _w((lambda *s: torch.randn(*s, dtype=torch.bfloat16) * 0.8)(D, Hq * Dh) * D ** (-0.5)), 'wo': _w((lambda *s: torch.randn(*s, dtype=torch.bfloat16) * 1.6)(Hq * Dh, D) * (Hq * Dh) ** (-0.5))}
    _A_in = _A_setup()

    def _B_setup():
        B, S, D, F = (1, 1, 512, 1792)
        return {'x': _w((lambda *s: torch.randn(*s, dtype=torch.bfloat16) * 0.1)(B, S, D)), 'w_gate': _w((lambda *s: torch.randn(*s, dtype=torch.bfloat16) * 0.2)(D, F)), 'w_up': _w((lambda *s: torch.randn(*s, dtype=torch.bfloat16) * 0.2)(D, F)), 'w_down': _w((lambda *s: torch.randn(*s, dtype=torch.bfloat16) * 0.2)(F, D))}
    _B_in = _B_setup()
    return {'A_x': _A_in['x'], 'A_k_cache': _A_in['k_cache'], 'A_v_cache': _A_in['v_cache'], 'A_wq': _A_in['wq'], 'A_wo': _A_in['wo'], 'B_x': _B_in['x'], 'B_w_gate': _B_in['w_gate'], 'B_w_up': _B_in['w_up'], 'B_w_down': _B_in['w_down'], 'c': _w((lambda *s: torch.randn(*s, dtype=torch.bfloat16) * 0.4)(1, 512) * 8.0), 'w_mod': _w((lambda *s: torch.randn(*s, dtype=torch.bfloat16) * 0.4)(512, 6 * 512) * 512 ** (-0.5) * 8.0)}

def reference(A_x, A_k_cache, A_v_cache, A_wq, A_wo, B_x, B_w_gate, B_w_up, B_w_down, c, w_mod):

    def _A_ref(x, k_cache, v_cache, wq, wo):
        B, Sq, D = x.shape
        Hq, Dh = (32, 128)
        Hkv, Sk = (k_cache.shape[1], k_cache.shape[2])
        W = min(4096, max(256, Sk // 4))
        rep = Hq // Hkv
        q = (lambda x, *d: x.permute(*d))((x @ wq).reshape(B, Sq, Hq, Dh), 0, 2, 1, 3)
        k = (lambda x, n, axis: torch.repeat_interleave(x, n, dim=axis))(k_cache[:, :, Sk - W:, :], rep, axis=1)
        v = (lambda x, n, axis: torch.repeat_interleave(x, n, dim=axis))(v_cache[:, :, Sk - W:, :], rep, axis=1)
        scores = torch.einsum('bhqd,bhkd->bhqk', q, k) * Dh ** (-0.5)
        p = _softmax(scores, axis=-1)
        o = torch.einsum('bhqk,bhkd->bhqd', p, v)
        return (lambda x, *d: x.permute(*d))(o, 0, 2, 1, 3).reshape(B, Sq, Hq * Dh) @ wo

    def _B_ref(x, w_gate, w_up, w_down):
        return _gelu(x @ w_gate) * (x @ w_up) @ w_down
    x = A_x
    mod = (_silu(c) @ w_mod).reshape(c.shape[0], 6, 1, -1)
    sa, ca, ga, sb, cb, gb = (mod[:, 0].clone(), mod[:, 1].clone(), mod[:, 2].clone(), mod[:, 3].clone(), mod[:, 4].clone(), mod[:, 5].clone())
    h = _layernorm(x, axis=-1) * (1 + ca) + sa
    x = x + ga * _A_ref(h, A_k_cache, A_v_cache, A_wq, A_wo)
    h = _layernorm(x, axis=-1) * (1 + cb) + sb
    x = x + gb * _B_ref(h, B_w_gate, B_w_up, B_w_down)
    return x
```

```cpp
#include <hip/hip_runtime.h>
#include <hip/hip_bf16.h>
#include <stdint.h>

typedef unsigned short u16;
__device__ __forceinline__ float bf2f(u16 b) { return __uint_as_float(((unsigned)b) << 16); }
__device__ __forceinline__ float rbf(float f) { return __bfloat162float(__float2bfloat16(f)); }

#define MIMIC 1
#define R(x) rbf(x)

constexpr int D = 512, HQ = 32, HKV = 8, SK = 65536, DH = 128, W = 4096, F = 1792;

__global__ void gemv_cols(const float* a, const u16* Wt, float* y, int K, int N) {
    int n = blockIdx.x * blockDim.x + threadIdx.x;
    if (n >= N) return;
    float acc = 0.f;
    for (int k = 0; k < K; ++k) acc = fmaf(a[k], bf2f(Wt[(size_t)k * N + n]), acc);
    y[n] = R(acc);
}
__global__ void k_silu(const u16* c, float* sc) {
    int i = threadIdx.x;
    float v = bf2f(c[i]);
    sc[i] = R(v / (1.f + __expf(-v)));
}
__global__ void k_x0(const u16* x, float* xf) { xf[threadIdx.x] = bf2f(x[threadIdx.x]); }
__global__ void k_ln(const float* x, const float* sh, const float* cs, float* h) {
    __shared__ float red[512];
    int i = threadIdx.x;
    float v = x[i];
    red[i] = v; __syncthreads();
    for (int s = 256; s > 0; s >>= 1) { if (i < s) red[i] += red[i + s]; __syncthreads(); }
    float mean = red[0] / 512.f; __syncthreads();
    float d = v - mean;
    red[i] = d * d; __syncthreads();
    for (int s = 256; s > 0; s >>= 1) { if (i < s) red[i] += red[i + s]; __syncthreads(); }
    float var = R(red[0] / 512.f);
    float m = R(mean);
    float num = R(v - m);
    float den = R(sqrtf(R(var + 1e-5f)));
    float ln = R(num / den);
    h[i] = R(R(ln * R(1.f + cs[i])) + sh[i]);
}
__global__ void k_attn(const float* q, const u16* kc, const u16* vc, float* o) {
    __shared__ float s[W];
    __shared__ float red[256];
    __shared__ float qs[DH];
    int h = blockIdx.x, g = h / 4, t = threadIdx.x;
    if (t < DH) qs[t] = q[h * DH + t];
    __syncthreads();
    const u16* kb = kc + ((size_t)g * SK + (SK - W)) * DH;
    const u16* vb = vc + ((size_t)g * SK + (SK - W)) * DH;
    float mx = -INFINITY;
    for (int j = t; j < W; j += 256) {
        float acc = 0.f;
        for (int d = 0; d < DH; ++d) acc = fmaf(qs[d], bf2f(kb[(size_t)j * DH + d]), acc);
        acc = R(R(acc) * 0.08838834764831845f);
        s[j] = acc; mx = fmaxf(mx, acc);
    }
    red[t] = mx; __syncthreads();
    for (int st = 128; st > 0; st >>= 1) { if (t < st) red[t] = fmaxf(red[t], red[t + st]); __syncthreads(); }
    mx = red[0]; __syncthreads();
    float sum = 0.f;
    for (int j = t; j < W; j += 256) { float e = __expf(s[j] - mx); s[j] = e; sum += e; }
    red[t] = sum; __syncthreads();
    for (int st = 128; st > 0; st >>= 1) { if (t < st) red[t] += red[t + st]; __syncthreads(); }
    sum = red[0]; __syncthreads();
    for (int j = t; j < W; j += 256) s[j] = R(s[j] / sum);
    __syncthreads();
    if (t < DH) {
        float acc = 0.f;
        for (int j = 0; j < W; ++j) acc = fmaf(s[j], bf2f(vb[(size_t)j * DH + t]), acc);
        o[h * DH + t] = R(acc);
    }
}
__global__ void k_res(const float* x, const float* g, const float* y, float* xo) {
    int i = threadIdx.x;
    xo[i] = R(x[i] + R(g[i] * y[i]));
}
__global__ void k_act(const float* g, const float* u, float* a) {
    int i = blockIdx.x * blockDim.x + threadIdx.x;
    if (i >= F) return;
    float v = g[i];
    float t = tanhf(0.7978845608028654f * (v + 0.044715f * v * v * v));
    a[i] = R(R(0.5f * v * (1.f + t)) * u[i]);
}
__global__ void k_out(const float* x, const float* g, const float* y, __hip_bfloat16* out) {
    int i = threadIdx.x;
    out[i] = __float2bfloat16(x[i] + R(g[i] * y[i]));
}

extern "C" void kernel_launch(void* const* d_in, const int* in_sizes, int n_in,
                              void* d_out, int out_size, void* d_ws, size_t ws_size,
                              hipStream_t stream) {
    const u16* x = (const u16*)d_in[0];
    const u16* kc = (const u16*)d_in[1];
    const u16* vc = (const u16*)d_in[2];
    const u16* wq = (const u16*)d_in[3];
    const u16* wo = (const u16*)d_in[4];
    const u16* wg = (const u16*)d_in[6];
    const u16* wu = (const u16*)d_in[7];
    const u16* wd = (const u16*)d_in[8];
    const u16* c = (const u16*)d_in[9];
    const u16* wm = (const u16*)d_in[10];
    float* ws = (float*)d_ws;
    float *sc = ws, *mod = ws + 512, *xf = mod + 3072, *h = xf + 512, *q = h + 512, *o = q + 4096, *ao = o + 4096,
          *x2 = ao + 512, *h2 = x2 + 512, *gg = h2 + 512, *uu = gg + F, *act = uu + F, *dn = act + F;
    k_silu<<<1, 512, 0, stream>>>(c, sc);
    k_x0<<<1, 512, 0, stream>>>(x, xf);
    gemv_cols<<<3072 / 64, 64, 0, stream>>>(sc, wm, mod, 512, 3072);
    k_ln<<<1, 512, 0, stream>>>(xf, mod, mod + 512, h);
    gemv_cols<<<4096 / 64, 64, 0, stream>>>(h, wq, q, 512, 4096);
    k_attn<<<32, 256, 0, stream>>>(q, kc, vc, o);
    gemv_cols<<<512 / 64, 64, 0, stream>>>(o, wo, ao, 4096, 512);
    k_res<<<1, 512, 0, stream>>>(xf, mod + 1024, ao, x2);
    k_ln<<<1, 512, 0, stream>>>(x2, mod + 1536, mod + 2048, h2);
    gemv_cols<<<F / 64, 64, 0, stream>>>(h2, wg, gg, 512, F);
    gemv_cols<<<F / 64, 64, 0, stream>>>(h2, wu, uu, 512, F);
    k_act<<<F / 256, 256, 0, stream>>>(gg, uu, act);
    gemv_cols<<<512 / 64, 64, 0, stream>>>(act, wd, dn, F, 512);
    k_out<<<1, 512, 0, stream>>>(x2, mod + 2560, dn, (__hip_bfloat16*)d_out);
}
```

```cpp
#include <hip/hip_runtime.h>
#include <stdint.h>
#pragma clang diagnostic ignored "-Wint-to-pointer-cast"

#define LAS __attribute__((address_space(3)))
typedef unsigned short u16;
typedef unsigned long long u64;
typedef short v4s __attribute__((ext_vector_type(4)));
typedef __bf16 v8bf __attribute__((ext_vector_type(8)));
typedef __bf16 v2bf __attribute__((ext_vector_type(2)));
typedef float v2f __attribute__((ext_vector_type(2)));
typedef float v4f __attribute__((ext_vector_type(4)));
typedef unsigned v4u __attribute__((ext_vector_type(4)));
typedef unsigned v2u __attribute__((ext_vector_type(2)));

#define STAMPS 0
#define KV_INTERLEAVE 1
#define Q_POLL_VM 5
#define F_DELAY 0

constexpr int SK = 65536, WIN = 4096, FF = 1792;
constexpr int WS_E = 0, WS_FLGA = 4096, WS_FLGF = 8192, WS_MOD = 16384, MOD_HALF = 1536 * 16, WS_Q = WS_MOD + 2 * MOD_HALF,
              WS_H3 = WS_Q + 16384, WS_ML = WS_H3 + 32 * 16 * 512, WS_H4 = WS_ML + 4096, WS_H6 = WS_H4 + 16 * 2048,
              WS_END = WS_H6 + 56 * 2048, WS_DBG = 1 << 20;
constexpr int L_T0 = 0, L_T1 = 65536, L_T2 = 98304, L_SM = 131072;
constexpr int L_Q = L_SM, L_P = L_Q + 4 * 320, L_ST = L_P + 4 * 576, L_O = L_ST + 256;
constexpr int L_ACT = L_SM, L_X2 = L_ACT + 1024, L_RED = L_X2 + 2048, L_A2 = L_RED + 128, L_SC = L_A2 + 64;
constexpr int L_TAG = L_SM + 5104, LDS_BYTES = L_SM + 5120;
constexpr unsigned SENT = 0xFFFFFFFFu;
constexpr int SPIN_CAP = 1 << 16;
constexpr float LOG2E = 1.4426950408889634f;

struct Params { const u16 *x, *kc, *vc, *wq, *wo, *wg, *wu, *wd, *c, *wm; u16* out; unsigned char* ws; };

__device__ __forceinline__ v4u mk_srd(const void* p, unsigned bytes) {
    const u64 a = (u64)p;
    return v4u{(unsigned)a, (unsigned)(a >> 32) & 0xffffu, bytes, 0x20000u};
}
template <int N, int LSTEP>
__device__ __forceinline__ void dma_seq(v4u srd, unsigned voff, unsigned soff0, unsigned gstep, unsigned lds0) {
    unsigned so;
    asm volatile(
        "s_nop 4\n\t"
        "s_mov_b32 m0, %[l]\n\t"
        "s_mov_b32 %[so], %[s0]\n\t"
        ".rept %c[n]\n\t"
        "buffer_load_dwordx4 %[vo], %[srd], %[so] offen nt lds\n\t"
        "s_add_u32 m0, m0, %c[ls]\n\t"
        "s_add_u32 %[so], %[so], %[gs]\n\t"
        ".endr"
        : [so] "=&s"(so)
        : [l] "s"(lds0), [s0] "s"(soff0), [n] "i"(N), [vo] "v"(voff), [srd] "s"(srd), [ls] "i"(LSTEP), [gs] "s"(gstep)
        : "memory", "scc");
}
#define WAIT_VM(n) asm volatile("s_waitcnt vmcnt(%0)" ::"i"(n) : "memory")
#define LD_HEAD "s_nop 0\n\t"
__device__ __forceinline__ unsigned opq(unsigned v) { asm("" : "+v"(v)); return v; }
__device__ __forceinline__ void pl1(v4u srd, unsigned vo, v4u& a) {
    asm volatile("buffer_load_dwordx4 %0, %1, %2, 0 offen sc1\n\ts_waitcnt vmcnt(0)" : "=&v"(a) : "v"(vo), "s"(srd) : "memory");
}
__device__ __forceinline__ void pl4(v4u srd, unsigned vo, unsigned s1, unsigned s2, unsigned s3, v4u& a, v4u& b, v4u& c, v4u& d) {
    asm volatile("buffer_load_dwordx4 %0, %4, %5, 0 offen sc1\n\tbuffer_load_dwordx4 %1, %4, %5, %6 offen sc1\n\t"
                 "buffer_load_dwordx4 %2, %4, %5, %7 offen sc1\n\tbuffer_load_dwordx4 %3, %4, %5, %8 offen sc1\n\ts_waitcnt vmcnt(0)"
                 : "=&v"(a), "=&v"(b), "=&v"(c), "=&v"(d) : "v"(vo), "s"(srd), "s"(s1), "s"(s2), "s"(s3) : "memory");
}
__device__ __forceinline__ bool any_lane(bool c) { return __builtin_amdgcn_ballot_w64(c) != 0; }
__device__ __forceinline__ unsigned tag_bad(v4u v, unsigned tlo, unsigned thi) { return (v.y ^ tlo) | (v.w ^ thi); }
#define ENDPGM() __builtin_amdgcn_endpgm()
#define SCHED() __builtin_amdgcn_sched_barrier(0)
#define BARRIER() asm volatile("s_waitcnt lgkmcnt(0)\n\ts_barrier" ::: "memory")
#define CLOB() asm volatile("" ::: "memory")
typedef __amdgpu_buffer_rsrc_t rsrc_t;
__device__ __forceinline__ v4u ld16(rsrc_t r, int off) { return __builtin_amdgcn_raw_buffer_load_b128(r, off, 0, 16); }
__device__ __forceinline__ v2u ld8(rsrc_t r, int off) { return __builtin_amdgcn_raw_buffer_load_b64(r, off, 0, 16); }
__device__ __forceinline__ void st16(rsrc_t r, int off, v4u v) { __builtin_amdgcn_raw_buffer_store_b128(v, r, off, 0, 16); }
__device__ __forceinline__ void st8(rsrc_t r, int off, v2u v) { __builtin_amdgcn_raw_buffer_store_b64(v, r, off, 0, 16); }
__device__ __forceinline__ float f_of(unsigned u) { return __builtin_bit_cast(float, u); }
__device__ __forceinline__ unsigned u_of(float f) { return __builtin_bit_cast(unsigned, f); }
__device__ __forceinline__ float rbf(float x) { return (float)(__bf16)x; }
__device__ __forceinline__ unsigned pk2(float a, float b) { return __builtin_bit_cast(unsigned, __builtin_convertvector(v2f{a, b}, v2bf)); }
__device__ __forceinline__ float lo16(unsigned u) { return f_of(u << 16); }
__device__ __forceinline__ float hi16(unsigned u) { return f_of(u & 0xffff0000u); }
template <int CTRL> __device__ __forceinline__ float dpp(float x) {
    return __builtin_bit_cast(float, __builtin_amdgcn_mov_dpp(__builtin_bit_cast(int, x), CTRL, 0xf, 0xf, true));
}
constexpr int X1 = 0xB1, X2 = 0x4E, ROR4 = 0x124, ROR8 = 0x128;
__device__ __forceinline__ float add16(float a, float b) { auto s = __builtin_amdgcn_permlane16_swap(u_of(a), u_of(b), false, false); return f_of(s[0]) + f_of(s[1]); }
__device__ __forceinline__ float add32(float a, float b) { auto s = __builtin_amdgcn_permlane32_swap(u_of(a), u_of(b), false, false); return f_of(s[0]) + f_of(s[1]); }
__device__ __forceinline__ float max16(float a) { auto s = __builtin_amdgcn_permlane16_swap(u_of(a), u_of(a), false, false); return fmaxf(f_of(s[0]), f_of(s[1])); }
__device__ __forceinline__ float max32(float a) { auto s = __builtin_amdgcn_permlane32_swap(u_of(a), u_of(a), false, false); return fmaxf(f_of(s[0]), f_of(s[1])); }
__device__ __forceinline__ float wave_sum(float v) {
    v += dpp<X1>(v); v += dpp<X2>(v); v += dpp<ROR4>(v); v += dpp<ROR8>(v); v = add16(v, v); return add32(v, v);
}
__device__ __forceinline__ float ex2(float x) { return __builtin_amdgcn_exp2f(x); }
__device__ __forceinline__ float fdiv(float n, float d) {
    const float r = __builtin_amdgcn_rcpf(d), q = n * r;
    return fmaf(fmaf(-q, d, n), r, q);
}
__device__ __forceinline__ float sigm(float x) { return __builtin_amdgcn_rcpf(1.f + ex2(-x * LOG2E)); }
__device__ __forceinline__ v8bf tr_frag(unsigned a0, unsigned a1) {
    const v4s t0 = __builtin_amdgcn_ds_read_tr16_b64_v4i16((LAS v4s*)a0), t1 = __builtin_amdgcn_ds_read_tr16_b64_v4i16((LAS v4s*)a1);
    return __builtin_bit_cast(v8bf, __builtin_shufflevector(t0, t1, 0, 1, 2, 3, 4, 5, 6, 7));
}
__device__ __forceinline__ v8bf lds_frag(unsigned a) { return *(LAS v8bf*)a; }
__device__ __forceinline__ v4f mfma(v8bf a, v8bf b, v4f c) { return __builtin_amdgcn_mfma_f32_16x16x32_bf16(a, b, c, 0, 0, 0); }
__device__ __forceinline__ bool tag_ok(v4u v, unsigned tlo, unsigned thi) { return v.y == tlo && v.w == thi; }
__device__ __forceinline__ unsigned umax4(v4u v) { return max(max(v.x, v.y), max(v.z, v.w)); }
__device__ __forceinline__ u64 rt() { return __builtin_amdgcn_s_memrealtime(); }

#define STAMP(k) do {} while (0)
#define STAMP_OUT() do {} while (0)

__device__ __forceinline__ unsigned tile_rd_addr(unsigned base, int w, int lane) {
    const int G = lane >> 4, q = (lane & 15) >> 2, p = lane & 3;
    return base + (8 * G + q) * 128 + (((2 * w + (p >> 1)) ^ (2 * ((q >> 1) | ((G & 1) << 1)))) << 4) + (p & 1) * 8;
}
__device__ __forceinline__ unsigned tile_dma_voff(int pitch, int w, int lane) {
    const int r8 = lane >> 3, p = lane & 7;
    return r8 * pitch + ((p ^ (2 * (((r8 >> 1) & 1) | ((w & 1) << 1)))) << 4);
}
__device__ __forceinline__ void silu_to_lds(unsigned cw, int tid) {
    const float a = lo16(cw), b = hi16(cw);
    *(LAS unsigned*)(L_SC + (tid & 127) * 4) = pk2(a * sigm(a), b * sigm(b));
}
__device__ __forceinline__ void mod_tile(unsigned base, rsrc_t wsr, int cb, int kh, int w, int lane, unsigned tlo, unsigned thi) {
    const unsigned ra = opq(tile_rd_addr(base, w, lane)), rb = opq(L_SC + 16 * (lane >> 4));
    v8bf wf[8], af[8];
#pragma unroll
    for (int t = 0; t < 8; ++t) { wf[t] = tr_frag(ra + t * 4096, ra + t * 4096 + 512); af[t] = lds_frag(rb + 64 * t); }
    SCHED();
    v4f d = {0.f, 0.f, 0.f, 0.f};
#pragma unroll
    for (int t = 0; t < 8; ++t) d = mfma(wf[t], af[t], d);
    const int e = lane & 15;
    if (e < 2) st16(wsr, WS_MOD + kh * MOD_HALF + (32 * cb + 8 * w + 2 * (lane >> 4) + e) * 16, v4u{u_of(e ? d.z : d.x), tlo, u_of(e ? d.w : d.y), thi});
}

#define TAG_FROM(ev) const u64 T_ = (((u64)(unsigned)__builtin_amdgcn_readfirstlane((ev).y) << 32) | (unsigned)__builtin_amdgcn_readfirstlane((ev).x)) + 1
__device__ __forceinline__ v2u tag_from_lds() {
    const v2u t = *(LAS v2u*)L_TAG;
    return v2u{(unsigned)__builtin_amdgcn_readfirstlane(t.x), (unsigned)__builtin_amdgcn_readfirstlane(t.y)};
}

__global__ void __launch_bounds__(512, 2) __attribute__((amdgpu_waves_per_eu(2, 2))) fused_block(Params p) {
    const int b = blockIdx.x, xcd = b & 7, ib = b >> 3, tid = threadIdx.x, lane = tid & 63;
    const int w = __builtin_amdgcn_readfirstlane(tid >> 6);
    const int G = lane >> 4, c16 = lane & 15;
    STAMP(0);
    const rsrc_t wsr = __builtin_amdgcn_make_buffer_rsrc(p.ws, 0, WS_END, 0x20000);
    const v4u wsd = mk_srd(p.ws, WS_END);
    if (ib < 16) {
        const int g = xcd, s = ib;
        if (w < 4) {
            const size_t hb = ((size_t)g * SK + (SK - WIN)) * 128;
            const unsigned so0 = (KV_INTERLEAVE ? s * 4096 : s * 65536) + w * 1024, gs = KV_INTERLEAVE ? 65536 : 4096;
            dma_seq<16, 4096>(mk_srd(p.kc + hb, 1 << 20), G * 256 + ((c16 ^ (4 * w + G)) << 4), so0, gs, L_T0 + w * 1024);
            dma_seq<16, 4096>(mk_srd(p.vc + hb, 1 << 20), G * 256 + ((c16 ^ (2 * G | ((w & 2) << 2))) << 4), so0, gs, L_T1 + w * 1024);
            STAMP(1);
            WAIT_VM(16);
        } else if (w == 7) {
            const v4u s4 = {SENT, SENT, SENT, SENT};
            st16(wsr, WS_H3 + ((4 * g + (lane >> 5)) * 16 + s) * 512 + (lane & 31) * 16, s4);
            st16(wsr, WS_H3 + ((4 * g + 2 + (lane >> 5)) * 16 + s) * 512 + (lane & 31) * 16, s4);
            if (lane < 4) st8(wsr, WS_ML + ((4 * g + lane) * 16 + s) * 8, v2u{SENT, SENT});
            if (lane < 16) st16(wsr, WS_H4 + (2 * g + (s >> 3)) * 2048 + (s & 7) * 256 + lane * 16, s4);
            v2u ev;
            asm volatile("buffer_load_dwordx2 %0, off, %1, 0 sc1\n\ts_waitcnt vmcnt(0)" : "=&v"(ev) : "s"(wsd) : "memory");
            TAG_FROM(ev);
            STAMP(1);
            if (lane == 0) { st16(wsr, WS_FLGA + (16 * g + s) * 16, v4u{0u, (unsigned)T_, 0u, (unsigned)(T_ >> 32)}); *(LAS v2u*)L_TAG = v2u{(unsigned)T_, (unsigned)(T_ >> 32)}; }
        }
        STAMP(2);
        BARRIER();
        v8bf kf[2][4];
#pragma unroll
        for (int t = 0; t < 4; ++t) {
            const unsigned a = L_T0 + (32 * w + c16) * 256 + (((4 * t + G) ^ c16) << 4);
            kf[0][t] = lds_frag(a); kf[1][t] = lds_frag(a + 4096);
        }
        const int ks = 2 * g + (s >> 3), j = s & 7;
        const unsigned qra = opq(L_Q + (lane & 3) * 320 + 16 * G), pra = opq(L_P + (lane & 3) * 576 + 16 * G), sra = opq(L_ST + (lane & 3) * 64);
        const unsigned vra = opq(L_T1 + (8 * G + (c16 >> 2)) * 256 + (((2 * w + ((lane & 3) >> 1)) ^ (2 * (c16 >> 2) | ((G & 1) << 3))) << 4) + (lane & 1) * 8);
        const unsigned pwa = opq(L_P + (lane & 3) * 576 + (32 * w + ((lane & 4) << 2) + 4 * G + ((lane & 8) >> 2)) * 2);
        const unsigned h3st = opq(WS_H3 + ((4 * g + (lane & 3)) * 16 + s) * 512 + (16 * w + 4 * G) * 4);
        const unsigned wovo = opq(tile_dma_voff(1024, w, lane));
        if (w < 4) {
            const v2u tg = tag_from_lds();
            const unsigned qoff = lane < 32 ? WS_Q + (32 * (4 * g + w) + lane) * 16 : WS_FLGA + (16 * g + c16) * 16;
            const unsigned qwa = opq(L_Q + w * 320 + (lane & 31) * 8);
            WAIT_VM(Q_POLL_VM);
            STAMP(11);
            v4u qv;
#pragma unroll 1
            for (int it = 0; it < SPIN_CAP; ++it) { pl1(wsd, qoff, qv); if (!any_lane(tag_bad(qv, tg.x, tg.y) != 0)) break; }
            if (lane < 32) *(LAS v2u*)qwa = v2u{qv.x, qv.z};
        }
        STAMP(3);
        BARRIER();
        STAMP(4);
        v8bf qf[4], vf[8];
#pragma unroll
        for (int t = 0; t < 4; ++t) qf[t] = lds_frag(qra + 64 * t);
        if (w < 4) dma_seq<8, 4096>(mk_srd(p.wo + (size_t)ks * 256 * 512 + 64 * j, 256 * 1024), wovo, w * 8192, 32768, L_T0 + w * 1024);
#pragma unroll
        for (int t = 0; t < 8; ++t) vf[t] = tr_frag(vra + t * 8192, vra + t * 8192 + 1024);
        SCHED();
        v4f d0 = {0.f, 0.f, 0.f, 0.f}, d1 = d0;
#pragma unroll
        for (int t = 0; t < 4; ++t) { d0 = mfma(kf[0][t], qf[t], d0); d1 = mfma(kf[1][t], qf[t], d1); }
        {
            const bool tau = lane & 4, pr = lane & 8;
            const float a0 = pr ? d0.z : d0.x, a1 = pr ? d0.w : d0.y, b0 = pr ? d1.z : d1.x, b1 = pr ? d1.w : d1.y;
            const float s0 = rbf(rbf(tau ? b0 : a0) * 0.08838834764831845f), s1 = rbf(rbf(tau ? b1 : a1) * 0.08838834764831845f);
            float m = fmaxf(s0, s1);
            m = fmaxf(m, dpp<ROR4>(m)); m = fmaxf(m, dpp<ROR8>(m)); m = max16(m); m = max32(m);
            const float p0 = ex2((s0 - m) * LOG2E), p1 = ex2((s1 - m) * LOG2E);
            *(LAS unsigned*)pwa = pk2(p0, p1);
            float l = p0 + p1;
            l += dpp<ROR4>(l); l += dpp<ROR8>(l); l = add16(l, l); l = add32(l, l);
            if (lane < 4) { *(LAS float*)(sra + w * 4) = m; *(LAS float*)(sra + 32 + w * 4) = l; }
        }
        STAMP(5);
        BARRIER();
        {
            v8bf pf[8];
            const v4f ma = *(const LAS v4f*)sra, mb = *(const LAS v4f*)(sra + 16);
#pragma unroll
            for (int t = 0; t < 8; ++t) pf[t] = lds_frag(pra + 64 * t);
            const v4f la = *(const LAS v4f*)(sra + 32), lb = *(const LAS v4f*)(sra + 48);
            SCHED();
            v4f dv[8];
#pragma unroll
            for (int t = 0; t < 8; ++t) dv[t] = mfma(vf[t], pf[t], v4f{0.f, 0.f, 0.f, 0.f});
            const float M = fmaxf(fmaxf(fmaxf(ma.x, ma.y), fmaxf(ma.z, ma.w)), fmaxf(fmaxf(mb.x, mb.y), fmaxf(mb.z, mb.w)));
            float f[8];
#pragma unroll
            for (int t = 0; t < 4; ++t) { f[t] = ex2((ma[t] - M) * LOG2E); f[4 + t] = ex2((mb[t] - M) * LOG2E); }
            v4f o = dv[0] * f[0];
#pragma unroll
            for (int t = 1; t < 8; ++t) o += dv[t] * f[t];
            if (c16 < 4) st16(wsr, h3st, __builtin_bit_cast(v4u, o));
            if (w == 4) {
                float L = 0.f;
#pragma unroll
                for (int t = 0; t < 4; ++t) L += f[t] * la[t] + f[4 + t] * lb[t];
                if (lane < 4) st8(wsr, WS_ML + ((4 * g + lane) * 16 + s) * 8, v2u{u_of(M), u_of(L)});
            }
        }
        STAMP(6);
        const int hd = 2 * ks + (w >> 2), s8 = lane >> 3;
        const unsigned o3 = WS_H3 + (hd * 16 + s8) * 512 + (w & 3) * 128 + (lane & 7) * 16, oml = WS_ML + (hd * 16 + s8) * 8;
        const unsigned owa = opq(L_O + ((w >> 2) * 128 + (w & 3) * 32 + 4 * (lane & 7) + G) * 2), ora = opq(L_O + 16 * G);
        const unsigned h4st = opq(WS_H4 + ks * 2048 + (64 * j + 16 * w + 4 * G) * 4);
        if (w < 4) WAIT_VM(1);
        BARRIER();
        STAMP(7);
        v8bf wf[8];
        {
            const unsigned ra = opq(tile_rd_addr(L_T0, w & 3, lane));
#pragma unroll
            for (int t = 0; t < 8; ++t) wf[t] = tr_frag(ra + t * 4096, ra + t * 4096 + 512);
        }
        v4u a0, a1; v2u m0, m1;
#pragma unroll 1
        for (int it = 0; it < SPIN_CAP; ++it) {
            asm volatile("buffer_load_dwordx2 %2, %5, %6, 0 offen sc1\n\tbuffer_load_dwordx2 %3, %5, %6, 0 offen offset:64 sc1\n\t"
                         "buffer_load_dwordx4 %0, %4, %6, 0 offen sc1\n\tbuffer_load_dwordx4 %1, %4, %6, %7 offen sc1\n\ts_waitcnt vmcnt(0)"
                         : "=&v"(a0), "=&v"(a1), "=&v"(m0), "=&v"(m1) : "v"(o3), "v"(oml), "s"(wsd), "s"(8 * 512) : "memory");
            const unsigned mx = max(max(umax4(a0), umax4(a1)), max(max(m0.x, m0.y), max(m1.x, m1.y)));
            if (!any_lane(mx == SENT)) break;
        }
        STAMP(8);
        {
            float M = fmaxf(f_of(m0.x), f_of(m1.x));
            M = fmaxf(M, dpp<ROR8>(M)); M = max16(M); M = max32(M);
            const float w0 = ex2((f_of(m0.x) - M) * LOG2E), w1 = ex2((f_of(m1.x) - M) * LOG2E);
            float L = w0 * f_of(m0.y) + w1 * f_of(m1.y);
            const v4f v = __builtin_bit_cast(v4f, a0) * w0 + __builtin_bit_cast(v4f, a1) * w1;
            L += dpp<ROR8>(L); L = add16(L, L); L = add32(L, L);
            float z = add16(add32(v.x, v.z), add32(v.y, v.w));
            z += dpp<ROR8>(z);
            *(LAS u16*)owa = (u16)pk2(fdiv(z, L), 0.f);
        }
        BARRIER();
        STAMP(9);
        if (w < 4) {
            v8bf of[8];
#pragma unroll
            for (int t = 0; t < 8; ++t) of[t] = lds_frag(ora + 64 * t);
            SCHED();
            v4f d = {0.f, 0.f, 0.f, 0.f};
#pragma unroll
            for (int t = 0; t < 8; ++t) d = mfma(wf[t], of[t], d);
            if (c16 == 0) st16(wsr, h4st, __builtin_bit_cast(v4u, d));
        }
        STAMP(10);
        STAMP_OUT();
        ENDPGM();
    }
    if (ib < 24) {
        const int g = xcd, j = ib - 16, mt = xcd * 8 + j, cb = 16 + (mt >> 1), kh = mt & 1;
        if (w < 4) {
            dma_seq<16, 4096>(mk_srd(p.wq + 512 * g + 64 * j, 512 * 8192), tile_dma_voff(8192, w, lane), w * 65536, 4 * 65536, L_T0 + w * 1024);
            STAMP(1);
            WAIT_VM(0);
            STAMP(2);
            BARRIER();
            v8bf wf[16];
            const unsigned ra = opq(tile_rd_addr(L_T0, w, lane)), rb = opq(L_ACT + 16 * G);
#pragma unroll
            for (int t = 0; t < 16; ++t) wf[t] = tr_frag(ra + t * 4096, ra + t * 4096 + 512);
            const v2u tg = tag_from_lds();
            const unsigned qst = opq(WS_Q + (128 * g + 16 * j + 4 * w + G) * 16), mvo = opq(tile_dma_voff(6144, w, lane));
            BARRIER();
            STAMP(4);
            v8bf hf[16];
#pragma unroll
            for (int t = 0; t < 16; ++t) hf[t] = lds_frag(rb + 64 * t);
            SCHED();
            v4f d = {0.f, 0.f, 0.f, 0.f};
#pragma unroll
            for (int t = 0; t < 16; ++t) d = mfma(wf[t], hf[t], d);
            if (c16 == 0) st16(wsr, qst, v4u{pk2(d.x, d.y), tg.x, pk2(d.z, d.w), tg.y});
            STAMP(5);
            dma_seq<8, 4096>(mk_srd(p.wm + (size_t)kh * 256 * 3072 + 64 * cb, 256 * 6144), mvo, w * 8 * 6144, 32 * 6144, L_T1 + w * 1024);
            WAIT_VM(0);
            BARRIER();
            STAMP(6);
            mod_tile(L_T1, wsr, cb, kh, w, lane, tg.x, tg.y);
            STAMP(7);
            STAMP_OUT();
            ENDPGM();
        }
        const int hp = 64 * (w & 3) + lane;
        v2u ev; v4u x8; unsigned x2w, cw;
        asm volatile("buffer_load_dwordx2 %0, off, %4, 0 sc1\n\tglobal_load_dwordx4 %1, %5, %8\n\tglobal_load_dword %2, %6, %8\n\tglobal_load_dword %3, %7, %9\n\ts_waitcnt vmcnt(0)"
                     : "=&v"(ev), "=&v"(x8), "=&v"(x2w), "=&v"(cw)
                     : "s"(wsd), "v"(lane * 16), "v"(hp * 4), "v"((128 * kh + (tid & 127)) * 4), "s"(p.x), "s"(p.c) : "memory");
        TAG_FROM(ev);
        const unsigned tlo = (unsigned)T_, thi = (unsigned)(T_ >> 32);
        *(LAS v2u*)L_TAG = v2u{tlo, thi};
        STAMP(1);
        silu_to_lds(cw, tid);
        float xs[8];
#pragma unroll
        for (int k = 0; k < 4; ++k) { xs[2 * k] = lo16(x8[k]); xs[2 * k + 1] = hi16(x8[k]); }
        float sum = 0.f;
#pragma unroll
        for (int k = 0; k < 8; ++k) sum += xs[k];
        const float mean = wave_sum(sum) * (1.f / 512.f);
        float sq = 0.f;
#pragma unroll
        for (int k = 0; k < 8; ++k) sq = fmaf(xs[k] - mean, xs[k] - mean, sq);
        const float var = rbf(wave_sum(sq) * (1.f / 512.f)), mr = rbf(mean), den = rbf(__builtin_amdgcn_sqrtf(rbf(var + 1e-5f)));
        const float ln0 = rbf(fdiv(rbf(lo16(x2w) - mr), den)), ln1 = rbf(fdiv(rbf(hi16(x2w) - mr), den));
        const unsigned hwa = opq(L_ACT + hp * 4);
        BARRIER();
        STAMP(2);
        v4u sa0, sa1, ca0, ca1;
#pragma unroll 1
        for (int it = 0; it < SPIN_CAP; ++it) {
            pl4(wsd, WS_MOD + hp * 16, MOD_HALF, 4096, 4096 + MOD_HALF, sa0, sa1, ca0, ca1);
            if (!any_lane((tag_bad(sa0, tlo, thi) | tag_bad(sa1, tlo, thi) | tag_bad(ca0, tlo, thi) | tag_bad(ca1, tlo, thi)) != 0)) break;
        }
        STAMP(3);
        const float h0 = rbf(ln0 * rbf(1.f + rbf(f_of(ca0.x) + f_of(ca1.x)))) + rbf(f_of(sa0.x) + f_of(sa1.x));
        const float h1 = rbf(ln1 * rbf(1.f + rbf(f_of(ca0.z) + f_of(ca1.z)))) + rbf(f_of(sa0.z) + f_of(sa1.z));
        *(LAS unsigned*)hwa = pk2(h0, h1);
        BARRIER();
        STAMP(4);
        STAMP_OUT();
        BARRIER();
        ENDPGM();
    }
    {
        const int u = ib - 24;
        const int f = xcd < 4 ? 8 * xcd + u : (u < 6 ? 32 + 6 * (xcd - 4) + u : -1);
        if (f < 0) ENDPGM();
        const u64 t_in = F_DELAY ? rt() : 0;
        const int mt = xcd * 4 + u, cb = mt >> 1, kh = mt & 1;
        if (w < 4) {
            if (u < 4) {
                dma_seq<8, 4096>(mk_srd(p.wm + (size_t)kh * 256 * 3072 + 64 * cb, 256 * 6144), tile_dma_voff(6144, w, lane), w * 8 * 6144, 32 * 6144, L_T2 + w * 1024);
                STAMP(1);
                WAIT_VM(0);
            }
        } else {
            v2u ev; unsigned cw;
            asm volatile("buffer_load_dwordx2 %0, off, %2, 0 sc1\n\tglobal_load_dword %1, %3, %4\n\ts_waitcnt vmcnt(0)"
                         : "=&v"(ev), "=&v"(cw) : "s"(wsd), "v"((128 * kh + (tid & 127)) * 4), "s"(p.c) : "memory");
            TAG_FROM(ev);
            *(LAS v2u*)L_TAG = v2u{(unsigned)T_, (unsigned)(T_ >> 32)};
            silu_to_lds(cw, tid);
            STAMP(1);
        }
        BARRIER();
        STAMP(2);
        const v2u tg = tag_from_lds();
        const unsigned tlo = tg.x, thi = tg.y;
        if (w < 4) { if (u < 4) mod_tile(L_T2, wsr, cb, kh, w, lane, tlo, thi); }
        else if (w == 7) {
            const v4u s4 = {SENT, SENT, SENT, SENT};
            st16(wsr, WS_H6 + f * 2048 + lane * 16, s4);
            st16(wsr, WS_H6 + f * 2048 + 1024 + lane * 16, s4);
        }
        BARRIER();
        STAMP(3);
        if (F_DELAY) { while (rt() - t_in < F_DELAY) __builtin_amdgcn_s_sleep(2); }
        const int col = 64 * w + 4 * c16 + G, colr = 32 * f + 4 * (lane & 7) + G;
        if (w < 4) {
            const unsigned vo = (lane >> 2) * 3584, pz = (lane & 3) ^ (lane >> 5);
            dma_seq<8, 4096>(mk_srd(p.wg + 32 * f, 512 * 3584), vo + (pz << 4), w * 16 * 3584, 64 * 3584, L_T0 + w * 1024);
            dma_seq<8, 4096>(mk_srd(p.wu + 32 * f, 512 * 3584), vo + ((pz ^ 2) << 4), w * 16 * 3584, 64 * 3584, L_T0 + 32768 + w * 1024);
            dma_seq<8, 4096>(mk_srd(p.wd + (size_t)32 * f * 512, 32 * 1024), (lane ^ (2 * w)) << 4, w * 1024, 4096, L_T2 + w * 1024);
        } else if (w == 7) {
            WAIT_VM(0);
            if (lane == 0) st16(wsr, WS_FLGF + f * 16, v4u{0u, tlo, 0u, thi});
        }
        BARRIER();
        STAMP(4);
        const bool red = f < 16 && w == 7;
        float ga, sb, cb1, gb, xi;
        {
            const unsigned om = WS_MOD + (512 + (col >> 1)) * 16, of = WS_FLGA + (16 * w + c16) * 16;
            const unsigned og = WS_MOD + (1280 + ((colr & 511) >> 1)) * 16, off = WS_FLGF + (lane < 56 ? lane : 0) * 16;
            v4u g0, g1, s0, s1, c0, c1, fa, b0, b1, fb; unsigned xc;
#pragma unroll 1
            for (int it = 0; it < SPIN_CAP; ++it) {
                asm volatile("buffer_load_dwordx4 %0, %11, %15, 0 offen sc1\n\tbuffer_load_dwordx4 %1, %11, %15, %16 offen sc1\n\t"
                             "buffer_load_dwordx4 %2, %11, %15, %17 offen sc1\n\tbuffer_load_dwordx4 %3, %11, %15, %18 offen sc1\n\t"
                             "buffer_load_dwordx4 %4, %11, %15, %19 offen sc1\n\tbuffer_load_dwordx4 %5, %11, %15, %20 offen sc1\n\t"
                             "buffer_load_dwordx4 %6, %12, %15, 0 offen sc1\n\tbuffer_load_dwordx4 %7, %13, %15, 0 offen sc1\n\t"
                             "buffer_load_dwordx4 %8, %13, %15, %16 offen sc1\n\tbuffer_load_dwordx4 %9, %14, %15, 0 offen sc1\n\t"
                             "global_load_ushort %10, %21, %22\n\ts_waitcnt vmcnt(0)"
                             : "=&v"(g0), "=&v"(g1), "=&v"(s0), "=&v"(s1), "=&v"(c0), "=&v"(c1), "=&v"(fa), "=&v"(b0), "=&v"(b1), "=&v"(fb), "=&v"(xc)
                             : "v"(om), "v"(of), "v"(og), "v"(off), "s"(wsd), "s"(MOD_HALF), "s"(4096), "s"(4096 + MOD_HALF), "s"(8192), "s"(8192 + MOD_HALF),
                               "v"(col * 2), "s"(p.x) : "memory");
                const unsigned bad = tag_bad(g0, tlo, thi) | tag_bad(g1, tlo, thi) | tag_bad(s0, tlo, thi) | tag_bad(s1, tlo, thi) | tag_bad(c0, tlo, thi) |
                                     tag_bad(c1, tlo, thi) | tag_bad(fa, tlo, thi) | tag_bad(b0, tlo, thi) | tag_bad(b1, tlo, thi) | tag_bad(fb, tlo, thi);
                if (!any_lane(bad != 0)) break;
                __builtin_amdgcn_s_sleep(2);
            }
            const bool od = G & 1;
            ga = rbf(od ? f_of(g0.z) + f_of(g1.z) : f_of(g0.x) + f_of(g1.x));
            sb = rbf(od ? f_of(s0.z) + f_of(s1.z) : f_of(s0.x) + f_of(s1.x));
            cb1 = rbf(1.f + rbf(od ? f_of(c0.z) + f_of(c1.z) : f_of(c0.x) + f_of(c1.x)));
            gb = rbf(od ? f_of(b0.z) + f_of(b1.z) : f_of(b0.x) + f_of(b1.x));
            xi = lo16(xc);
        }
        STAMP(5);
        BARRIER();
        STAMP(6);
        v8bf gf[16], df[4];
        {
            const int q = c16 >> 2, pp = lane & 3;
            const unsigned ra = opq(L_T0 + (pp >> 1) * 32768 + (8 * G + q) * 64 + (((w & 3) ^ ((G & 1) | (pp & 2))) << 4) + (pp & 1) * 8);
#pragma unroll
            for (int t = 0; t < 16; ++t) gf[t] = tr_frag(ra + t * 2048, ra + t * 2048 + 256);
#pragma unroll
            for (int t = 0; t < 4; ++t) {
                const unsigned rd = opq(L_T2 + (8 * G + q) * 1024 + ((8 * w + ((2 * t) ^ (2 * q)) + (pp >> 1)) << 4) + (pp & 1) * 8);
                df[t] = tr_frag(rd, rd + 4096);
            }
        }
        const unsigned x2wa = opq(L_X2 + col * 4), hwa = opq(L_ACT + col * 2), hra = opq(L_ACT + 16 * G), awa = opq(L_A2 + (8 * w + 4 * (G & 1) + (lane & 3)) * 2);
        const unsigned ara = opq(L_A2 + 16 * G), h6st = opq(WS_H6 + f * 2048 + (64 * w + 4 * G) * 4), rwa = opq(L_RED + w * 4), rra = opq(L_RED);
        v4u a0, a1, a2, a3;
#pragma unroll 1
        for (int it = 0; it < SPIN_CAP; ++it) {
            pl4(wsd, WS_H4 + G * 2048 + (64 * w + 4 * c16) * 4, 4 * 2048, 8 * 2048, 12 * 2048, a0, a1, a2, a3);
            const unsigned mx = max(max(umax4(a0), umax4(a1)), max(umax4(a2), umax4(a3)));
            if (!any_lane(mx == SENT)) break;
        }
        STAMP(7);
        const v4f sv = (__builtin_bit_cast(v4f, a0) + __builtin_bit_cast(v4f, a1)) + (__builtin_bit_cast(v4f, a2) + __builtin_bit_cast(v4f, a3));
        const float ao = rbf(add16(add32(sv.x, sv.z), add32(sv.y, sv.w)));
        const float x2 = rbf(xi + rbf(ga * ao));
        {
            const float s1 = wave_sum(x2), s2 = wave_sum(x2 * x2);
            *(LAS float*)rwa = s1; *(LAS float*)(rwa + 32) = s2;
            *(LAS float*)x2wa = x2;
        }
        BARRIER();
        STAMP(8);
        {
            const LAS v4f* rp = (const LAS v4f*)rra;
            const v4f r0 = rp[0] + rp[1], r1 = rp[2] + rp[3];
            const float mean = ((r0.x + r0.y) + (r0.z + r0.w)) * (1.f / 512.f), ex = ((r1.x + r1.y) + (r1.z + r1.w)) * (1.f / 512.f);
            const float var = rbf(fmaxf(fmaf(-mean, mean, ex), 0.f)), den = rbf(__builtin_amdgcn_sqrtf(rbf(var + 1e-5f)));
            const float ln = rbf(fdiv(rbf(x2 - rbf(mean)), den));
            *(LAS u16*)hwa = (u16)pk2(rbf(ln * cb1) + sb, 0.f);
        }
        BARRIER();
        STAMP(9);
        if (w < 4) {
            v8bf hf[16];
#pragma unroll
            for (int t = 0; t < 16; ++t) hf[t] = lds_frag(hra + 64 * t);
            SCHED();
            v4f d = {0.f, 0.f, 0.f, 0.f};
#pragma unroll
            for (int t = 0; t < 16; ++t) d = mfma(gf[t], hf[t], d);
            const float v = (lane & 2) ? ((lane & 1) ? d.w : d.z) : ((lane & 1) ? d.y : d.x);
            const auto sw = __builtin_amdgcn_permlane32_swap(u_of(v), u_of(v), false, false);
            const float gt = rbf(f_of(sw[0])), up = rbf(f_of(sw[1]));
            const float ge = rbf(gt * sigm(1.5957691216057308f * fmaf(0.044715f * gt * gt, gt, gt)));
            *(LAS u16*)awa = (u16)pk2(ge * up, 0.f);
        }
        BARRIER();
        STAMP(10);
        {
            const v8bf af = lds_frag(ara);
            v4f d[4];
#pragma unroll
            for (int t = 0; t < 4; ++t) d[t] = mfma(df[t], af, v4f{0.f, 0.f, 0.f, 0.f});
            if (c16 == 0) {
#pragma unroll
                for (int t = 0; t < 4; ++t) st16(wsr, h6st + 64 * t, __builtin_bit_cast(v4u, d[t]));
            }
        }
        STAMP(11);
        if (red) {
            const float xr = *(LAS float*)(L_X2 + colr * 4);
            const unsigned o6 = WS_H6 + (lane >> 3) * 2048 + (32 * f + 4 * (lane & 7)) * 4;
            v4u r0, r1, r2, r3, r4, r5, r6;
#pragma unroll 1
            for (int it = 0; it < SPIN_CAP; ++it) {
                asm volatile("buffer_load_dwordx4 %0, %7, %8, 0 offen sc1\n\tbuffer_load_dwordx4 %1, %7, %8, %9 offen sc1\n\t"
                             "buffer_load_dwordx4 %2, %7, %8, %10 offen sc1\n\tbuffer_load_dwordx4 %3, %7, %8, %11 offen sc1\n\t"
                             "buffer_load_dwordx4 %4, %7, %8, %12 offen sc1\n\tbuffer_load_dwordx4 %5, %7, %8, %13 offen sc1\n\t"
                             "buffer_load_dwordx4 %6, %7, %8, %14 offen sc1\n\ts_waitcnt vmcnt(0)"
                             : "=&v"(r0), "=&v"(r1), "=&v"(r2), "=&v"(r3), "=&v"(r4), "=&v"(r5), "=&v"(r6)
                             : "v"(o6), "s"(wsd), "s"(16384), "s"(2 * 16384), "s"(3 * 16384), "s"(4 * 16384), "s"(5 * 16384), "s"(6 * 16384) : "memory");
                const unsigned mx = max(max(max(umax4(r0), umax4(r1)), max(umax4(r2), umax4(r3))), max(max(umax4(r4), umax4(r5)), umax4(r6)));
                if (!any_lane(mx == SENT)) break;
            }
            STAMP(12);
            const v4f v = ((__builtin_bit_cast(v4f, r0) + __builtin_bit_cast(v4f, r1)) + (__builtin_bit_cast(v4f, r2) + __builtin_bit_cast(v4f, r3))) +
                          ((__builtin_bit_cast(v4f, r4) + __builtin_bit_cast(v4f, r5)) + __builtin_bit_cast(v4f, r6));
            float z = add16(add32(v.x, v.z), add32(v.y, v.w));
            z += dpp<ROR8>(z);
            if (!(lane & 8)) p.out[colr] = (u16)pk2(xr + rbf(gb * rbf(z)), 0.f);
            if (f == 0 && lane == 0) st8(wsr, WS_E, v2u{tlo, thi});
            STAMP(13);
        }
        STAMP_OUT();
    }
}

extern "C" void kernel_launch(void* const* d_in, const int* in_sizes, int n_in, void* d_out, int out_size, void* d_ws, size_t ws_size,
                              hipStream_t stream) {
    Params p;
    p.x = (const u16*)d_in[0]; p.kc = (const u16*)d_in[1]; p.vc = (const u16*)d_in[2]; p.wq = (const u16*)d_in[3]; p.wo = (const u16*)d_in[4];
    p.wg = (const u16*)d_in[6]; p.wu = (const u16*)d_in[7]; p.wd = (const u16*)d_in[8]; p.c = (const u16*)d_in[9]; p.wm = (const u16*)d_in[10];
    p.out = (u16*)d_out; p.ws = (unsigned char*)d_ws;
    (void)hipFuncSetAttribute((const void*)fused_block, hipFuncAttributeMaxDynamicSharedMemorySize, LDS_BYTES);
    hipLaunchKernelGGL(fused_block, dim3(256), dim3(512), LDS_BYTES, stream, p);
}
```

```cpp
#include <hip/hip_runtime.h>
#include <stdint.h>
#pragma clang diagnostic ignored "-Wint-to-pointer-cast"

#define LAS __attribute__((address_space(3)))
typedef unsigned short u16;
typedef unsigned long long u64;
typedef short v4s __attribute__((ext_vector_type(4)));
typedef __bf16 v8bf __attribute__((ext_vector_type(8)));
typedef __bf16 v2bf __attribute__((ext_vector_type(2)));
typedef float v2f __attribute__((ext_vector_type(2)));
typedef float v4f __attribute__((ext_vector_type(4)));
typedef unsigned v4u __attribute__((ext_vector_type(4)));
typedef unsigned v2u __attribute__((ext_vector_type(2)));

#define STAMPS 0
#define KV_INTERLEAVE 1
#define Q_POLL_VM 5
#define F_DELAY 0

constexpr int SK = 65536, WIN = 4096, FF = 1792;
constexpr int WS_E = 0, WS_FLGA = 4096, WS_FLGF = 8192, WS_MOD = 16384, MOD_HALF = 1536 * 16, WS_Q = WS_MOD + 2 * MOD_HALF,
              WS_H3 = WS_Q + 16384, WS_ML = WS_H3 + 32 * 16 * 512, WS_H4 = WS_ML + 4096, WS_H6 = WS_H4 + 16 * 2048,
              WS_EXP = WS_H6 + 56 * 2048, WS_END = WS_EXP + 3 * 4096, WS_DBG = 1 << 20;
constexpr int L_T0 = 0, L_T1 = 65536, L_T2 = 98304, L_SM = 131072;
constexpr int L_Q = L_SM, L_P = L_Q + 4 * 320, L_ST = L_P + 4 * 576, L_O = L_ST + 256;
constexpr int L_ACT = L_SM, L_X2 = L_ACT + 1024, L_RED = L_X2 + 2048, L_A2 = L_RED + 128, L_SC = L_A2 + 64;
constexpr int L_TAG = L_SM + 5104, LDS_BYTES = L_SM + 5120;
constexpr unsigned SENT = 0xFFFFFFFFu;
constexpr int SPIN_CAP = 1 << 16;
constexpr float LOG2E = 1.4426950408889634f;

struct Params { const u16 *x, *kc, *vc, *wq, *wo, *wg, *wu, *wd, *c, *wm; u16* out; unsigned char* ws; };

__device__ __forceinline__ v4u mk_srd(const void* p, unsigned bytes) {
    const u64 a = (u64)p;
    return v4u{(unsigned)a, (unsigned)(a >> 32) & 0xffffu, bytes, 0x20000u};
}
template <int N, int LSTEP>
__device__ __forceinline__ void dma_seq(v4u srd, unsigned voff, unsigned soff0, unsigned gstep, unsigned lds0) {
    unsigned so;
    asm volatile(
        "s_nop 4\n\t"
        "s_mov_b32 m0, %[l]\n\t"
        "s_mov_b32 %[so], %[s0]\n\t"
        ".rept %c[n]\n\t"
        "buffer_load_dwordx4 %[vo], %[srd], %[so] offen nt lds\n\t"
        "s_add_u32 m0, m0, %c[ls]\n\t"
        "s_add_u32 %[so], %[so], %[gs]\n\t"
        ".endr"
        : [so] "=&s"(so)
        : [l] "s"(lds0), [s0] "s"(soff0), [n] "i"(N), [vo] "v"(voff), [srd] "s"(srd), [ls] "i"(LSTEP), [gs] "s"(gstep)
        : "memory", "scc");
}
#define WAIT_VM(n) asm volatile("s_waitcnt vmcnt(%0)" ::"i"(n) : "memory")
#define LD_HEAD "s_nop 0\n\t"
__device__ __forceinline__ unsigned opq(unsigned v) { asm("" : "+v"(v)); return v; }
__device__ __forceinline__ void pl1(v4u srd, unsigned vo, v4u& a) {
    asm volatile("buffer_load_dwordx4 %0, %1, %2, 0 offen sc1\n\ts_waitcnt vmcnt(0)" : "=&v"(a) : "v"(vo), "s"(srd) : "memory");
}
__device__ __forceinline__ void pl4(v4u srd, unsigned vo, unsigned s1, unsigned s2, unsigned s3, v4u& a, v4u& b, v4u& c, v4u& d) {
    asm volatile("buffer_load_dwordx4 %0, %4, %5, 0 offen sc1\n\tbuffer_load_dwordx4 %1, %4, %5, %6 offen sc1\n\t"
                 "buffer_load_dwordx4 %2, %4, %5, %7 offen sc1\n\tbuffer_load_dwordx4 %3, %4, %5, %8 offen sc1\n\ts_waitcnt vmcnt(0)"
                 : "=&v"(a), "=&v"(b), "=&v"(c), "=&v"(d) : "v"(vo), "s"(srd), "s"(s1), "s"(s2), "s"(s3) : "memory");
}
__device__ __forceinline__ bool any_lane(bool c) { return __builtin_amdgcn_ballot_w64(c) != 0; }
__device__ __forceinline__ unsigned tag_bad(v4u v, unsigned tlo, unsigned thi) { return (v.y ^ tlo) | (v.w ^ thi); }
#define ENDPGM() __builtin_amdgcn_endpgm()
#define SCHED() __builtin_amdgcn_sched_barrier(0)
#define BARRIER() asm volatile("s_waitcnt lgkmcnt(0)\n\ts_barrier" ::: "memory")
#define CLOB() asm volatile("" ::: "memory")
typedef __amdgpu_buffer_rsrc_t rsrc_t;
__device__ __forceinline__ v4u ld16(rsrc_t r, int off) { return __builtin_amdgcn_raw_buffer_load_b128(r, off, 0, 16); }
__device__ __forceinline__ v2u ld8(rsrc_t r, int off) { return __builtin_amdgcn_raw_buffer_load_b64(r, off, 0, 16); }
__device__ __forceinline__ void st16(rsrc_t r, int off, v4u v) { __builtin_amdgcn_raw_buffer_store_b128(v, r, off, 0, 16); }
__device__ __forceinline__ void st8(rsrc_t r, int off, v2u v) { __builtin_amdgcn_raw_buffer_store_b64(v, r, off, 0, 16); }
__device__ __forceinline__ float f_of(unsigned u) { return __builtin_bit_cast(float, u); }
__device__ __forceinline__ unsigned u_of(float f) { return __builtin_bit_cast(unsigned, f); }
__device__ __forceinline__ float rbf(float x) { return (float)(__bf16)x; }
__device__ __forceinline__ unsigned pk2(float a, float b) { return __builtin_bit_cast(unsigned, __builtin_convertvector(v2f{a, b}, v2bf)); }
__device__ __forceinline__ float lo16(unsigned u) { return f_of(u << 16); }
__device__ __forceinline__ float hi16(unsigned u) { return f_of(u & 0xffff0000u); }
template <int CTRL> __device__ __forceinline__ float dpp(float x) {
    return __builtin_bit_cast(float, __builtin_amdgcn_mov_dpp(__builtin_bit_cast(int, x), CTRL, 0xf, 0xf, true));
}
constexpr int X1 = 0xB1, X2 = 0x4E, ROR4 = 0x124, ROR8 = 0x128;
__device__ __forceinline__ float add16(float a, float b) { auto s = __builtin_amdgcn_permlane16_swap(u_of(a), u_of(b), false, false); return f_of(s[0]) + f_of(s[1]); }
__device__ __forceinline__ float add32(float a, float b) { auto s = __builtin_amdgcn_permlane32_swap(u_of(a), u_of(b), false, false); return f_of(s[0]) + f_of(s[1]); }
__device__ __forceinline__ float max16(float a) { auto s = __builtin_amdgcn_permlane16_swap(u_of(a), u_of(a), false, false); return fmaxf(f_of(s[0]), f_of(s[1])); }
__device__ __forceinline__ float max32(float a) { auto s = __builtin_amdgcn_permlane32_swap(u_of(a), u_of(a), false, false); return fmaxf(f_of(s[0]), f_of(s[1])); }
__device__ __forceinline__ float wave_sum(float v) {
    v += dpp<X1>(v); v += dpp<X2>(v); v += dpp<ROR4>(v); v += dpp<ROR8>(v); v = add16(v, v); return add32(v, v);
}
__device__ __forceinline__ float ex2(float x) { return __builtin_amdgcn_exp2f(x); }
__device__ __forceinline__ float fdiv(float n, float d) {
    const float r = __builtin_amdgcn_rcpf(d), q = n * r;
    return fmaf(fmaf(-q, d, n), r, q);
}
__device__ __forceinline__ float sigm(float x) { return __builtin_amdgcn_rcpf(1.f + ex2(-x * LOG2E)); }
__device__ __forceinline__ v8bf tr_frag(unsigned a0, unsigned a1) {
    const v4s t0 = __builtin_amdgcn_ds_read_tr16_b64_v4i16((LAS v4s*)a0), t1 = __builtin_amdgcn_ds_read_tr16_b64_v4i16((LAS v4s*)a1);
    return __builtin_bit_cast(v8bf, __builtin_shufflevector(t0, t1, 0, 1, 2, 3, 4, 5, 6, 7));
}
__device__ __forceinline__ v8bf lds_frag(unsigned a) { return *(LAS v8bf*)a; }
__device__ __forceinline__ v4f mfma(v8bf a, v8bf b, v4f c) { return __builtin_amdgcn_mfma_f32_16x16x32_bf16(a, b, c, 0, 0, 0); }
__device__ __forceinline__ bool tag_ok(v4u v, unsigned tlo, unsigned thi) { return v.y == tlo && v.w == thi; }
__device__ __forceinline__ unsigned umax4(v4u v) { return max(max(v.x, v.y), max(v.z, v.w)); }
__device__ __forceinline__ u64 rt() { return __builtin_amdgcn_s_memrealtime(); }

#define STAMP(k) do {} while (0)
#define STAMP_OUT() do {} while (0)

__device__ __forceinline__ unsigned tile_rd_addr(unsigned base, int w, int lane) {
    const int G = lane >> 4, q = (lane & 15) >> 2, p = lane & 3;
    return base + (8 * G + q) * 128 + (((2 * w + (p >> 1)) ^ (2 * ((q >> 1) | ((G & 1) << 1)))) << 4) + (p & 1) * 8;
}
__device__ __forceinline__ unsigned tile_dma_voff(int pitch, int w, int lane) {
    const int r8 = lane >> 3, p = lane & 7;
    return r8 * pitch + ((p ^ (2 * (((r8 >> 1) & 1) | ((w & 1) << 1)))) << 4);
}
__device__ __forceinline__ void silu_to_lds(unsigned cw, int tid) {
    const float a = lo16(cw), b = hi16(cw);
    *(LAS unsigned*)(L_SC + (tid & 127) * 4) = pk2(a * sigm(a), b * sigm(b));
}
__device__ __forceinline__ void mod_tile(unsigned base, rsrc_t wsr, int cb, int kh, int w, int lane, unsigned tlo, unsigned thi) {
    const unsigned ra = opq(tile_rd_addr(base, w, lane)), rb = opq(L_SC + 16 * (lane >> 4));
    v8bf wf[8], af[8];
#pragma unroll
    for (int t = 0; t < 8; ++t) { wf[t] = tr_frag(ra + t * 4096, ra + t * 4096 + 512); af[t] = lds_frag(rb + 64 * t); }
    SCHED();
    v4f d = {0.f, 0.f, 0.f, 0.f};
#pragma unroll
    for (int t = 0; t < 8; ++t) d = mfma(wf[t], af[t], d);
    const int e = lane & 15;
    if (e < 2) st16(wsr, WS_MOD + kh * MOD_HALF + (32 * cb + 8 * w + 2 * (lane >> 4) + e) * 16, v4u{u_of(e ? d.z : d.x), tlo, u_of(e ? d.w : d.y), thi});
}

#define TAG_FROM(ev) const u64 T_ = (((u64)(unsigned)__builtin_amdgcn_readfirstlane((ev).y) << 32) | (unsigned)__builtin_amdgcn_readfirstlane((ev).x)) + 1
__device__ __forceinline__ v2u tag_from_lds() {
    const v2u t = *(LAS v2u*)L_TAG;
    return v2u{(unsigned)__builtin_amdgcn_readfirstlane(t.x), (unsigned)__builtin_amdgcn_readfirstlane(t.y)};
}

__global__ void __launch_bounds__(512, 2) __attribute__((amdgpu_waves_per_eu(2, 2))) fused_block(Params p) {
    const int b = blockIdx.x, xcd = b & 7, ib = b >> 3, tid = threadIdx.x, lane = tid & 63;
    const int w = __builtin_amdgcn_readfirstlane(tid >> 6);
    const int G = lane >> 4, c16 = lane & 15;
    STAMP(0);
    const u64 t_start = rt();
    const rsrc_t wsr = __builtin_amdgcn_make_buffer_rsrc(p.ws, 0, WS_END, 0x20000);
    const v4u wsd = mk_srd(p.ws, WS_END);
    if (ib < 16) {
        const int g = xcd, s = ib;
        if (w < 4) {
            const size_t hb = ((size_t)g * SK + (SK - WIN)) * 128;
            const unsigned so0 = (KV_INTERLEAVE ? s * 4096 : s * 65536) + w * 1024, gs = KV_INTERLEAVE ? 65536 : 4096;
            dma_seq<16, 4096>(mk_srd(p.kc + hb, 1 << 20), G * 256 + ((c16 ^ (4 * w + G)) << 4), so0, gs, L_T0 + w * 1024);
            dma_seq<16, 4096>(mk_srd(p.vc + hb, 1 << 20), G * 256 + ((c16 ^ (2 * G | ((w & 2) << 2))) << 4), so0, gs, L_T1 + w * 1024);
            STAMP(1);
            WAIT_VM(16);
        } else if (w == 7) {
            const v4u s4 = {SENT, SENT, SENT, SENT};
            st16(wsr, WS_H3 + ((4 * g + (lane >> 5)) * 16 + s) * 512 + (lane & 31) * 16, s4);
            st16(wsr, WS_H3 + ((4 * g + 2 + (lane >> 5)) * 16 + s) * 512 + (lane & 31) * 16, s4);
            if (lane < 4) st8(wsr, WS_ML + ((4 * g + lane) * 16 + s) * 8, v2u{SENT, SENT});
            if (lane < 16) st16(wsr, WS_H4 + (2 * g + (s >> 3)) * 2048 + (s & 7) * 256 + lane * 16, s4);
            v2u ev;
            asm volatile("buffer_load_dwordx2 %0, off, %1, 0 sc1\n\ts_waitcnt vmcnt(0)" : "=&v"(ev) : "s"(wsd) : "memory");
            TAG_FROM(ev);
            STAMP(1);
            if (lane == 0) { st16(wsr, WS_FLGA + (16 * g + s) * 16, v4u{0u, (unsigned)T_, 0u, (unsigned)(T_ >> 32)}); *(LAS v2u*)L_TAG = v2u{(unsigned)T_, (unsigned)(T_ >> 32)}; }
        }
        STAMP(2);
        BARRIER();
        v8bf kf[2][4];
#pragma unroll
        for (int t = 0; t < 4; ++t) {
            const unsigned a = L_T0 + (32 * w + c16) * 256 + (((4 * t + G) ^ c16) << 4);
            kf[0][t] = lds_frag(a); kf[1][t] = lds_frag(a + 4096);
        }
        const int ks = 2 * g + (s >> 3), j = s & 7;
        const unsigned qra = opq(L_Q + (lane & 3) * 320 + 16 * G), pra = opq(L_P + (lane & 3) * 576 + 16 * G), sra = opq(L_ST + (lane & 3) * 64);
        const unsigned vra = opq(L_T1 + (8 * G + (c16 >> 2)) * 256 + (((2 * w + ((lane & 3) >> 1)) ^ (2 * (c16 >> 2) | ((G & 1) << 3))) << 4) + (lane & 1) * 8);
        const unsigned pwa = opq(L_P + (lane & 3) * 576 + (32 * w + ((lane & 4) << 2) + 4 * G + ((lane & 8) >> 2)) * 2);
        const unsigned h3st = opq(WS_H3 + ((4 * g + (lane & 3)) * 16 + s) * 512 + (16 * w + 4 * G) * 4);
        const unsigned wovo = opq(tile_dma_voff(1024, w, lane));
        if (w < 4) {
            const v2u tg = tag_from_lds();
            const unsigned qoff = lane < 32 ? WS_Q + (32 * (4 * g + w) + lane) * 16 : WS_FLGA + (16 * g + c16) * 16;
            const unsigned qwa = opq(L_Q + w * 320 + (lane & 31) * 8);
            WAIT_VM(Q_POLL_VM);
            STAMP(11);
            v4u qv;
#pragma unroll 1
            for (int it = 0; it < SPIN_CAP; ++it) { pl1(wsd, qoff, qv); if (!any_lane(tag_bad(qv, tg.x, tg.y) != 0)) break; }
            if (lane < 32) *(LAS v2u*)qwa = v2u{qv.x, qv.z};
        }
        STAMP(3);
        BARRIER();
        STAMP(4);
        v8bf qf[4], vf[8];
#pragma unroll
        for (int t = 0; t < 4; ++t) qf[t] = lds_frag(qra + 64 * t);
        if (w < 4) dma_seq<8, 4096>(mk_srd(p.wo + (size_t)ks * 256 * 512 + 64 * j, 256 * 1024), wovo, w * 8192, 32768, L_T0 + w * 1024);
#pragma unroll
        for (int t = 0; t < 8; ++t) vf[t] = tr_frag(vra + t * 8192, vra + t * 8192 + 1024);
        SCHED();
        v4f d0 = {0.f, 0.f, 0.f, 0.f}, d1 = d0;
#pragma unroll
        for (int t = 0; t < 4; ++t) { d0 = mfma(kf[0][t], qf[t], d0); d1 = mfma(kf[1][t], qf[t], d1); }
        {
            const bool tau = lane & 4, pr = lane & 8;
            const float a0 = pr ? d0.z : d0.x, a1 = pr ? d0.w : d0.y, b0 = pr ? d1.z : d1.x, b1 = pr ? d1.w : d1.y;
            const float s0 = rbf(rbf(tau ? b0 : a0) * 0.08838834764831845f), s1 = rbf(rbf(tau ? b1 : a1) * 0.08838834764831845f);
            float m = fmaxf(s0, s1);
            m = fmaxf(m, dpp<ROR4>(m)); m = fmaxf(m, dpp<ROR8>(m)); m = max16(m); m = max32(m);
            const float p0 = ex2((s0 - m) * LOG2E), p1 = ex2((s1 - m) * LOG2E);
            *(LAS unsigned*)pwa = pk2(p0, p1);
            float l = p0 + p1;
            l += dpp<ROR4>(l); l += dpp<ROR8>(l); l = add16(l, l); l = add32(l, l);
            if (lane < 4) { *(LAS float*)(sra + w * 4) = m; *(LAS float*)(sra + 32 + w * 4) = l; }
        }
        STAMP(5);
        BARRIER();
        {
            v8bf pf[8];
            const v4f ma = *(const LAS v4f*)sra, mb = *(const LAS v4f*)(sra + 16);
#pragma unroll
            for (int t = 0; t < 8; ++t) pf[t] = lds_frag(pra + 64 * t);
            const v4f la = *(const LAS v4f*)(sra + 32), lb = *(const LAS v4f*)(sra + 48);
            SCHED();
            v4f dv[8];
#pragma unroll
            for (int t = 0; t < 8; ++t) dv[t] = mfma(vf[t], pf[t], v4f{0.f, 0.f, 0.f, 0.f});
            const float M = fmaxf(fmaxf(fmaxf(ma.x, ma.y), fmaxf(ma.z, ma.w)), fmaxf(fmaxf(mb.x, mb.y), fmaxf(mb.z, mb.w)));
            float f[8];
#pragma unroll
            for (int t = 0; t < 4; ++t) { f[t] = ex2((ma[t] - M) * LOG2E); f[4 + t] = ex2((mb[t] - M) * LOG2E); }
            v4f o = dv[0] * f[0];
#pragma unroll
            for (int t = 1; t < 8; ++t) o += dv[t] * f[t];
            if (c16 < 4) st16(wsr, h3st, __builtin_bit_cast(v4u, o));
            if (w == 4) {
                float L = 0.f;
#pragma unroll
                for (int t = 0; t < 4; ++t) L += f[t] * la[t] + f[4 + t] * lb[t];
                if (lane < 4) st8(wsr, WS_ML + ((4 * g + lane) * 16 + s) * 8, v2u{u_of(M), u_of(L)});
            }
        }
        STAMP(6);
        const int hd = 2 * ks + (w >> 2), s8 = lane >> 3;
        const unsigned o3 = WS_H3 + (hd * 16 + s8) * 512 + (w & 3) * 128 + (lane & 7) * 16, oml = WS_ML + (hd * 16 + s8) * 8;
        const unsigned owa = opq(L_O + ((w >> 2) * 128 + (w & 3) * 32 + 4 * (lane & 7) + G) * 2), ora = opq(L_O + 16 * G);
        const unsigned h4st = opq(WS_H4 + ks * 2048 + (64 * j + 16 * w + 4 * G) * 4);
        if (w < 4) WAIT_VM(1);
        BARRIER();
        STAMP(7);
        v8bf wf[8];
        {
            const unsigned ra = opq(tile_rd_addr(L_T0, w & 3, lane));
#pragma unroll
            for (int t = 0; t < 8; ++t) wf[t] = tr_frag(ra + t * 4096, ra + t * 4096 + 512);
        }
        v4u a0, a1; v2u m0, m1;
#pragma unroll 1
        for (int it = 0; it < SPIN_CAP; ++it) {
            asm volatile("buffer_load_dwordx2 %2, %5, %6, 0 offen sc1\n\tbuffer_load_dwordx2 %3, %5, %6, 0 offen offset:64 sc1\n\t"
                         "buffer_load_dwordx4 %0, %4, %6, 0 offen sc1\n\tbuffer_load_dwordx4 %1, %4, %6, %7 offen sc1\n\ts_waitcnt vmcnt(0)"
                         : "=&v"(a0), "=&v"(a1), "=&v"(m0), "=&v"(m1) : "v"(o3), "v"(oml), "s"(wsd), "s"(8 * 512) : "memory");
            const unsigned mx = max(max(umax4(a0), umax4(a1)), max(max(m0.x, m0.y), max(m1.x, m1.y)));
            if (!any_lane(mx == SENT)) break;
        }
        STAMP(8);
        {
            float M = fmaxf(f_of(m0.x), f_of(m1.x));
            M = fmaxf(M, dpp<ROR8>(M)); M = max16(M); M = max32(M);
            const float w0 = ex2((f_of(m0.x) - M) * LOG2E), w1 = ex2((f_of(m1.x) - M) * LOG2E);
            float L = w0 * f_of(m0.y) + w1 * f_of(m1.y);
            const v4f v = __builtin_bit_cast(v4f, a0) * w0 + __builtin_bit_cast(v4f, a1) * w1;
            L += dpp<ROR8>(L); L = add16(L, L); L = add32(L, L);
            float z = add16(add32(v.x, v.z), add32(v.y, v.w));
            z += dpp<ROR8>(z);
            *(LAS u16*)owa = (u16)pk2(fdiv(z, L), 0.f);
        }
        BARRIER();
        STAMP(9);
        if (w < 4) {
            v8bf of[8];
#pragma unroll
            for (int t = 0; t < 8; ++t) of[t] = lds_frag(ora + 64 * t);
            SCHED();
            v4f d = {0.f, 0.f, 0.f, 0.f};
#pragma unroll
            for (int t = 0; t < 8; ++t) d = mfma(wf[t], of[t], d);
            if (c16 == 0) st16(wsr, h4st, __builtin_bit_cast(v4u, d));
        }
        if (w == 0) { const v2u tq_ = tag_from_lds(); const u64 tn_ = rt(); if (lane == 0) { st16(wsr, WS_EXP + b * 16, v4u{(unsigned)t_start, tq_.x, 0u, tq_.y}); st16(wsr, WS_EXP + 4096 + b * 16, v4u{(unsigned)tn_, tq_.x, 0u, tq_.y}); } }
        ENDPGM();
    }
    if (ib < 24) {
        const int g = xcd, j = ib - 16, mt = xcd * 8 + j, cb = 16 + (mt >> 1), kh = mt & 1;
        if (w < 4) {
            dma_seq<16, 4096>(mk_srd(p.wq + 512 * g + 64 * j, 512 * 8192), tile_dma_voff(8192, w, lane), w * 65536, 4 * 65536, L_T0 + w * 1024);
            STAMP(1);
            WAIT_VM(0);
            STAMP(2);
            BARRIER();
            v8bf wf[16];
            const unsigned ra = opq(tile_rd_addr(L_T0, w, lane)), rb = opq(L_ACT + 16 * G);
#pragma unroll
            for (int t = 0; t < 16; ++t) wf[t] = tr_frag(ra + t * 4096, ra + t * 4096 + 512);
            const v2u tg = tag_from_lds();
            const unsigned qst = opq(WS_Q + (128 * g + 16 * j + 4 * w + G) * 16), mvo = opq(tile_dma_voff(6144, w, lane));
            BARRIER();
            STAMP(4);
            v8bf hf[16];
#pragma unroll
            for (int t = 0; t < 16; ++t) hf[t] = lds_frag(rb + 64 * t);
            SCHED();
            v4f d = {0.f, 0.f, 0.f, 0.f};
#pragma unroll
            for (int t = 0; t < 16; ++t) d = mfma(wf[t], hf[t], d);
            if (c16 == 0) st16(wsr, qst, v4u{pk2(d.x, d.y), tg.x, pk2(d.z, d.w), tg.y});
            STAMP(5);
            dma_seq<8, 4096>(mk_srd(p.wm + (size_t)kh * 256 * 3072 + 64 * cb, 256 * 6144), mvo, w * 8 * 6144, 32 * 6144, L_T1 + w * 1024);
            WAIT_VM(0);
            BARRIER();
            STAMP(6);
            mod_tile(L_T1, wsr, cb, kh, w, lane, tg.x, tg.y);
            if (w == 0) { const v2u tq_ = tag_from_lds(); const u64 tn_ = rt(); if (lane == 0) { st16(wsr, WS_EXP + b * 16, v4u{(unsigned)t_start, tq_.x, 0u, tq_.y}); st16(wsr, WS_EXP + 4096 + b * 16, v4u{(unsigned)tn_, tq_.x, 0u, tq_.y}); } }
            ENDPGM();
        }
        const int hp = 64 * (w & 3) + lane;
        v2u ev; v4u x8; unsigned x2w, cw;
        asm volatile("buffer_load_dwordx2 %0, off, %4, 0 sc1\n\tglobal_load_dwordx4 %1, %5, %8\n\tglobal_load_dword %2, %6, %8\n\tglobal_load_dword %3, %7, %9\n\ts_waitcnt vmcnt(0)"
                     : "=&v"(ev), "=&v"(x8), "=&v"(x2w), "=&v"(cw)
                     : "s"(wsd), "v"(lane * 16), "v"(hp * 4), "v"((128 * kh + (tid & 127)) * 4), "s"(p.x), "s"(p.c) : "memory");
        TAG_FROM(ev);
        const unsigned tlo = (unsigned)T_, thi = (unsigned)(T_ >> 32);
        *(LAS v2u*)L_TAG = v2u{tlo, thi};
        STAMP(1);
        silu_to_lds(cw, tid);
        float xs[8];
#pragma unroll
        for (int k = 0; k < 4; ++k) { xs[2 * k] = lo16(x8[k]); xs[2 * k + 1] = hi16(x8[k]); }
        float sum = 0.f;
#pragma unroll
        for (int k = 0; k < 8; ++k) sum += xs[k];
        const float mean = wave_sum(sum) * (1.f / 512.f);
        float sq = 0.f;
#pragma unroll
        for (int k = 0; k < 8; ++k) sq = fmaf(xs[k] - mean, xs[k] - mean, sq);
        const float var = rbf(wave_sum(sq) * (1.f / 512.f)), mr = rbf(mean), den = rbf(__builtin_amdgcn_sqrtf(rbf(var + 1e-5f)));
        const float ln0 = rbf(fdiv(rbf(lo16(x2w) - mr), den)), ln1 = rbf(fdiv(rbf(hi16(x2w) - mr), den));
        const unsigned hwa = opq(L_ACT + hp * 4);
        BARRIER();
        STAMP(2);
        v4u sa0, sa1, ca0, ca1;
#pragma unroll 1
        for (int it = 0; it < SPIN_CAP; ++it) {
            pl4(wsd, WS_MOD + hp * 16, MOD_HALF, 4096, 4096 + MOD_HALF, sa0, sa1, ca0, ca1);
            if (!any_lane((tag_bad(sa0, tlo, thi) | tag_bad(sa1, tlo, thi) | tag_bad(ca0, tlo, thi) | tag_bad(ca1, tlo, thi)) != 0)) break;
        }
        STAMP(3);
        const float h0 = rbf(ln0 * rbf(1.f + rbf(f_of(ca0.x) + f_of(ca1.x)))) + rbf(f_of(sa0.x) + f_of(sa1.x));
        const float h1 = rbf(ln1 * rbf(1.f + rbf(f_of(ca0.z) + f_of(ca1.z)))) + rbf(f_of(sa0.z) + f_of(sa1.z));
        *(LAS unsigned*)hwa = pk2(h0, h1);
        BARRIER();
        STAMP(4);
        STAMP_OUT();
        BARRIER();
        ENDPGM();
    }
    {
        const int u = ib - 24;
        const int f = xcd < 4 ? 8 * xcd + u : (u < 6 ? 32 + 6 * (xcd - 4) + u : -1);
        if (f < 0) ENDPGM();
        const u64 t_in = F_DELAY ? rt() : 0;
        const int mt = xcd * 4 + u, cb = mt >> 1, kh = mt & 1;
        if (w < 4) {
            if (u < 4) {
                dma_seq<8, 4096>(mk_srd(p.wm + (size_t)kh * 256 * 3072 + 64 * cb, 256 * 6144), tile_dma_voff(6144, w, lane), w * 8 * 6144, 32 * 6144, L_T2 + w * 1024);
                STAMP(1);
                WAIT_VM(0);
            }
        } else {
            v2u ev; unsigned cw;
            asm volatile("buffer_load_dwordx2 %0, off, %2, 0 sc1\n\tglobal_load_dword %1, %3, %4\n\ts_waitcnt vmcnt(0)"
                         : "=&v"(ev), "=&v"(cw) : "s"(wsd), "v"((128 * kh + (tid & 127)) * 4), "s"(p.c) : "memory");
            TAG_FROM(ev);
            *(LAS v2u*)L_TAG = v2u{(unsigned)T_, (unsigned)(T_ >> 32)};
            silu_to_lds(cw, tid);
            STAMP(1);
        }
        BARRIER();
        STAMP(2);
        const v2u tg = tag_from_lds();
        const unsigned tlo = tg.x, thi = tg.y;
        if (w < 4) { if (u < 4) mod_tile(L_T2, wsr, cb, kh, w, lane, tlo, thi); }
        else if (w == 7) {
            const v4u s4 = {SENT, SENT, SENT, SENT};
            st16(wsr, WS_H6 + f * 2048 + lane * 16, s4);
            st16(wsr, WS_H6 + f * 2048 + 1024 + lane * 16, s4);
        }
        BARRIER();
        STAMP(3);
        if (F_DELAY) { while (rt() - t_in < F_DELAY) __builtin_amdgcn_s_sleep(2); }
        const int col = 64 * w + 4 * c16 + G, colr = 32 * f + 4 * (lane & 7) + G;
        if (w < 4) {
            const unsigned vo = (lane >> 2) * 3584, pz = (lane & 3) ^ (lane >> 5);
            dma_seq<8, 4096>(mk_srd(p.wg + 32 * f, 512 * 3584), vo + (pz << 4), w * 16 * 3584, 64 * 3584, L_T0 + w * 1024);
            dma_seq<8, 4096>(mk_srd(p.wu + 32 * f, 512 * 3584), vo + ((pz ^ 2) << 4), w * 16 * 3584, 64 * 3584, L_T0 + 32768 + w * 1024);
            dma_seq<8, 4096>(mk_srd(p.wd + (size_t)32 * f * 512, 32 * 1024), (lane ^ (2 * w)) << 4, w * 1024, 4096, L_T2 + w * 1024);
        } else if (w == 7) {
            WAIT_VM(0);
            if (lane == 0) st16(wsr, WS_FLGF + f * 16, v4u{0u, tlo, 0u, thi});
        }
        BARRIER();
        STAMP(4);
        const bool red = f < 16 && w == 7;
        float ga, sb, cb1, gb, xi;
        {
            const unsigned om = WS_MOD + (512 + (col >> 1)) * 16, of = WS_FLGA + (16 * w + c16) * 16;
            const unsigned og = WS_MOD + (1280 + ((colr & 511) >> 1)) * 16, off = WS_FLGF + (lane < 56 ? lane : 0) * 16;
            v4u g0, g1, s0, s1, c0, c1, fa, b0, b1, fb; unsigned xc;
#pragma unroll 1
            for (int it = 0; it < SPIN_CAP; ++it) {
                asm volatile("buffer_load_dwordx4 %0, %11, %15, 0 offen sc1\n\tbuffer_load_dwordx4 %1, %11, %15, %16 offen sc1\n\t"
                             "buffer_load_dwordx4 %2, %11, %15, %17 offen sc1\n\tbuffer_load_dwordx4 %3, %11, %15, %18 offen sc1\n\t"
                             "buffer_load_dwordx4 %4, %11, %15, %19 offen sc1\n\tbuffer_load_dwordx4 %5, %11, %15, %20 offen sc1\n\t"
                             "buffer_load_dwordx4 %6, %12, %15, 0 offen sc1\n\tbuffer_load_dwordx4 %7, %13, %15, 0 offen sc1\n\t"
                             "buffer_load_dwordx4 %8, %13, %15, %16 offen sc1\n\tbuffer_load_dwordx4 %9, %14, %15, 0 offen sc1\n\t"
                             "global_load_ushort %10, %21, %22\n\ts_waitcnt vmcnt(0)"
                             : "=&v"(g0), "=&v"(g1), "=&v"(s0), "=&v"(s1), "=&v"(c0), "=&v"(c1), "=&v"(fa), "=&v"(b0), "=&v"(b1), "=&v"(fb), "=&v"(xc)
                             : "v"(om), "v"(of), "v"(og), "v"(off), "s"(wsd), "s"(MOD_HALF), "s"(4096), "s"(4096 + MOD_HALF), "s"(8192), "s"(8192 + MOD_HALF),
                               "v"(col * 2), "s"(p.x) : "memory");
                const unsigned bad = tag_bad(g0, tlo, thi) | tag_bad(g1, tlo, thi) | tag_bad(s0, tlo, thi) | tag_bad(s1, tlo, thi) | tag_bad(c0, tlo, thi) |
                                     tag_bad(c1, tlo, thi) | tag_bad(fa, tlo, thi) | tag_bad(b0, tlo, thi) | tag_bad(b1, tlo, thi) | tag_bad(fb, tlo, thi);
                if (!any_lane(bad != 0)) break;
                __builtin_amdgcn_s_sleep(2);
            }
            const bool od = G & 1;
            ga = rbf(od ? f_of(g0.z) + f_of(g1.z) : f_of(g0.x) + f_of(g1.x));
            sb = rbf(od ? f_of(s0.z) + f_of(s1.z) : f_of(s0.x) + f_of(s1.x));
            cb1 = rbf(1.f + rbf(od ? f_of(c0.z) + f_of(c1.z) : f_of(c0.x) + f_of(c1.x)));
            gb = rbf(od ? f_of(b0.z) + f_of(b1.z) : f_of(b0.x) + f_of(b1.x));
            xi = lo16(xc);
        }
        STAMP(5);
        BARRIER();
        STAMP(6);
        v8bf gf[16], df[4];
        {
            const int q = c16 >> 2, pp = lane & 3;
            const unsigned ra = opq(L_T0 + (pp >> 1) * 32768 + (8 * G + q) * 64 + (((w & 3) ^ ((G & 1) | (pp & 2))) << 4) + (pp & 1) * 8);
#pragma unroll
            for (int t = 0; t < 16; ++t) gf[t] = tr_frag(ra + t * 2048, ra + t * 2048 + 256);
#pragma unroll
            for (int t = 0; t < 4; ++t) {
                const unsigned rd = opq(L_T2 + (8 * G + q) * 1024 + ((8 * w + ((2 * t) ^ (2 * q)) + (pp >> 1)) << 4) + (pp & 1) * 8);
                df[t] = tr_frag(rd, rd + 4096);
            }
        }
        const unsigned x2wa = opq(L_X2 + col * 4), hwa = opq(L_ACT + col * 2), hra = opq(L_ACT + 16 * G), awa = opq(L_A2 + (8 * w + 4 * (G & 1) + (lane & 3)) * 2);
        const unsigned ara = opq(L_A2 + 16 * G), h6st = opq(WS_H6 + f * 2048 + (64 * w + 4 * G) * 4), rwa = opq(L_RED + w * 4), rra = opq(L_RED);
        v4u a0, a1, a2, a3;
#pragma unroll 1
        for (int it = 0; it < SPIN_CAP; ++it) {
            pl4(wsd, WS_H4 + G * 2048 + (64 * w + 4 * c16) * 4, 4 * 2048, 8 * 2048, 12 * 2048, a0, a1, a2, a3);
            const unsigned mx = max(max(umax4(a0), umax4(a1)), max(umax4(a2), umax4(a3)));
            if (!any_lane(mx == SENT)) break;
        }
        STAMP(7);
        const v4f sv = (__builtin_bit_cast(v4f, a0) + __builtin_bit_cast(v4f, a1)) + (__builtin_bit_cast(v4f, a2) + __builtin_bit_cast(v4f, a3));
        const float ao = rbf(add16(add32(sv.x, sv.z), add32(sv.y, sv.w)));
        const float x2 = rbf(xi + rbf(ga * ao));
        {
            const float s1 = wave_sum(x2), s2 = wave_sum(x2 * x2);
            *(LAS float*)rwa = s1; *(LAS float*)(rwa + 32) = s2;
            *(LAS float*)x2wa = x2;
        }
        BARRIER();
        STAMP(8);
        {
            const LAS v4f* rp = (const LAS v4f*)rra;
            const v4f r0 = rp[0] + rp[1], r1 = rp[2] + rp[3];
            const float mean = ((r0.x + r0.y) + (r0.z + r0.w)) * (1.f / 512.f), ex = ((r1.x + r1.y) + (r1.z + r1.w)) * (1.f / 512.f);
            const float var = rbf(fmaxf(fmaf(-mean, mean, ex), 0.f)), den = rbf(__builtin_amdgcn_sqrtf(rbf(var + 1e-5f)));
            const float ln = rbf(fdiv(rbf(x2 - rbf(mean)), den));
            *(LAS u16*)hwa = (u16)pk2(rbf(ln * cb1) + sb, 0.f);
        }
        BARRIER();
        STAMP(9);
        if (w < 4) {
            v8bf hf[16];
#pragma unroll
            for (int t = 0; t < 16; ++t) hf[t] = lds_frag(hra + 64 * t);
            SCHED();
            v4f d = {0.f, 0.f, 0.f, 0.f};
#pragma unroll
            for (int t = 0; t < 16; ++t) d = mfma(gf[t], hf[t], d);
            const float v = (lane & 2) ? ((lane & 1) ? d.w : d.z) : ((lane & 1) ? d.y : d.x);
            const auto sw = __builtin_amdgcn_permlane32_swap(u_of(v), u_of(v), false, false);
            const float gt = rbf(f_of(sw[0])), up = rbf(f_of(sw[1]));
            const float ge = rbf(gt * sigm(1.5957691216057308f * fmaf(0.044715f * gt * gt, gt, gt)));
            *(LAS u16*)awa = (u16)pk2(ge * up, 0.f);
        }
        BARRIER();
        STAMP(10);
        {
            const v8bf af = lds_frag(ara);
            v4f d[4];
#pragma unroll
            for (int t = 0; t < 4; ++t) d[t] = mfma(df[t], af, v4f{0.f, 0.f, 0.f, 0.f});
            if (c16 == 0) {
#pragma unroll
                for (int t = 0; t < 4; ++t) st16(wsr, h6st + 64 * t, __builtin_bit_cast(v4u, d[t]));
            }
        }
        if (w == 0) { const v2u tq_ = tag_from_lds(); const u64 tn_ = rt(); if (lane == 0) { st16(wsr, WS_EXP + b * 16, v4u{(unsigned)t_start, tq_.x, 0u, tq_.y}); st16(wsr, WS_EXP + 4096 + b * 16, v4u{(unsigned)tn_, tq_.x, 0u, tq_.y}); } }
        if (red) {
            const float xr = *(LAS float*)(L_X2 + colr * 4);
            const unsigned o6 = WS_H6 + (lane >> 3) * 2048 + (32 * f + 4 * (lane & 7)) * 4;
            v4u r0, r1, r2, r3, r4, r5, r6;
#pragma unroll 1
            for (int it = 0; it < SPIN_CAP; ++it) {
                asm volatile("buffer_load_dwordx4 %0, %7, %8, 0 offen sc1\n\tbuffer_load_dwordx4 %1, %7, %8, %9 offen sc1\n\t"
                             "buffer_load_dwordx4 %2, %7, %8, %10 offen sc1\n\tbuffer_load_dwordx4 %3, %7, %8, %11 offen sc1\n\t"
                             "buffer_load_dwordx4 %4, %7, %8, %12 offen sc1\n\tbuffer_load_dwordx4 %5, %7, %8, %13 offen sc1\n\t"
                             "buffer_load_dwordx4 %6, %7, %8, %14 offen sc1\n\ts_waitcnt vmcnt(0)"
                             : "=&v"(r0), "=&v"(r1), "=&v"(r2), "=&v"(r3), "=&v"(r4), "=&v"(r5), "=&v"(r6)
                             : "v"(o6), "s"(wsd), "s"(16384), "s"(2 * 16384), "s"(3 * 16384), "s"(4 * 16384), "s"(5 * 16384), "s"(6 * 16384) : "memory");
                const unsigned mx = max(max(max(umax4(r0), umax4(r1)), max(umax4(r2), umax4(r3))), max(max(umax4(r4), umax4(r5)), umax4(r6)));
                if (!any_lane(mx == SENT)) break;
            }
            STAMP(12);
            const v4f v = ((__builtin_bit_cast(v4f, r0) + __builtin_bit_cast(v4f, r1)) + (__builtin_bit_cast(v4f, r2) + __builtin_bit_cast(v4f, r3))) +
                          ((__builtin_bit_cast(v4f, r4) + __builtin_bit_cast(v4f, r5)) + __builtin_bit_cast(v4f, r6));
            float z = add16(add32(v.x, v.z), add32(v.y, v.w));
            z += dpp<ROR8>(z);
            if (!(lane & 8)) p.out[colr] = (u16)pk2(xr + rbf(gb * rbf(z)), 0.f);
            if (f == 0 && lane == 0) st8(wsr, WS_E, v2u{tlo, thi});
            if (f == 0) {
                unsigned X = 0;
                const unsigned t0 = (unsigned)t_start;
                if (3 == 1) X = (unsigned)rt() - t0;
                if (3 >= 2) {
                    int mn = 1 << 30, mx = -(1 << 30);
                    for (int k = 0; k < 4; ++k) {
                        const int bb = k * 64 + lane; const bool val = !((bb >> 3) >= 30 && (bb & 7) >= 4);
                        const bool sel = 3 == 2 || 3 == 3 ? true : 3 == 4 ? (bb >> 3) < 16 : 3 == 5 ? ((bb >> 3) >= 16 && (bb >> 3) < 24) : true;
                        v4u e;
                        for (int it = 0; it < SPIN_CAP; ++it) { pl1(wsd, WS_EXP + (3 >= 4 ? 4096 : 0) + bb * 16, e); if (!any_lane(val && tag_bad(e, tlo, thi) != 0)) break; }
                        if (val && sel) { const int d = (int)(e.x - t0); mn = min(mn, d); mx = max(mx, d); }
                    }
                    for (int o = 32; o; o >>= 1) { mn = min(mn, __shfl_xor(mn, o)); mx = max(mx, __shfl_xor(mx, o)); }
                    X = 3 == 3 ? -mn : mx;
                    X = __builtin_amdgcn_readfirstlane(X);
                }
                while ((unsigned)rt() - t0 < 4000u + X) __builtin_amdgcn_s_sleep(1);
            }
        }
        STAMP_OUT();
    }
}

extern "C" void kernel_launch(void* const* d_in, const int* in_sizes, int n_in, void* d_out, int out_size, void* d_ws, size_t ws_size,
                              hipStream_t stream) {
    Params p;
    p.x = (const u16*)d_in[0]; p.kc = (const u16*)d_in[1]; p.vc = (const u16*)d_in[2]; p.wq = (const u16*)d_in[3]; p.wo = (const u16*)d_in[4];
    p.wg = (const u16*)d_in[6]; p.wu = (const u16*)d_in[7]; p.wd = (const u16*)d_in[8]; p.c = (const u16*)d_in[9]; p.wm = (const u16*)d_in[10];
    p.out = (u16*)d_out; p.ws = (unsigned char*)d_ws;
    (void)hipFuncSetAttribute((const void*)fused_block, hipFuncAttributeMaxDynamicSharedMemorySize, LDS_BYTES);
    hipLaunchKernelGGL(fused_block, dim3(256), dim3(512), LDS_BYTES, stream, p);
}
```

```cpp
#include <hip/hip_runtime.h>
#include <stdint.h>
#pragma clang diagnostic ignored "-Wint-to-pointer-cast"

#define LAS __attribute__((address_space(3)))
typedef unsigned short u16;
typedef unsigned long long u64;
typedef short v4s __attribute__((ext_vector_type(4)));
typedef __bf16 v8bf __attribute__((ext_vector_type(8)));
typedef __bf16 v2bf __attribute__((ext_vector_type(2)));
typedef float v2f __attribute__((ext_vector_type(2)));
typedef float v4f __attribute__((ext_vector_type(4)));
typedef unsigned v4u __attribute__((ext_vector_type(4)));
typedef unsigned v2u __attribute__((ext_vector_type(2)));

#define STAMPS 0
#define KV_INTERLEAVE 1
#define Q_POLL_VM 5
#define F_DELAY 0

constexpr int SK = 65536, WIN = 4096, FF = 1792;
constexpr int WS_E = 0, WS_FLGA = 4096, WS_FLGF = 8192, WS_MOD = 16384, MOD_HALF = 1536 * 16, WS_Q = WS_MOD + 2 * MOD_HALF,
              WS_H3 = WS_Q + 16384, WS_ML = WS_H3 + 32 * 16 * 512, WS_H4 = WS_ML + 4096, WS_H6 = WS_H4 + 16 * 2048,
              WS_EXP = WS_H6 + 56 * 2048, WS_END = WS_EXP + 3 * 4096, WS_DBG = 1 << 20;
constexpr int L_T0 = 0, L_T1 = 65536, L_T2 = 98304, L_SM = 131072;
constexpr int L_Q = L_SM, L_P = L_Q + 4 * 320, L_ST = L_P + 4 * 576, L_O = L_ST + 256;
constexpr int L_ACT = L_SM, L_X2 = L_ACT + 1024, L_RED = L_X2 + 2048, L_A2 = L_RED + 128, L_SC = L_A2 + 64;
constexpr int L_TAG = L_SM + 5104, LDS_BYTES = L_SM + 5120;
constexpr unsigned SENT = 0xFFFFFFFFu;
constexpr int SPIN_CAP = 1 << 16;
constexpr float LOG2E = 1.4426950408889634f;

struct Params { const u16 *x, *kc, *vc, *wq, *wo, *wg, *wu, *wd, *c, *wm; u16* out; unsigned char* ws; };

__device__ __forceinline__ v4u mk_srd(const void* p, unsigned bytes) {
    const u64 a = (u64)p;
    return v4u{(unsigned)a, (unsigned)(a >> 32) & 0xffffu, bytes, 0x20000u};
}
template <int N, int LSTEP>
__device__ __forceinline__ void dma_seq(v4u srd, unsigned voff, unsigned soff0, unsigned gstep, unsigned lds0) {
    unsigned so;
    asm volatile(
        "s_nop 4\n\t"
        "s_mov_b32 m0, %[l]\n\t"
        "s_mov_b32 %[so], %[s0]\n\t"
        ".rept %c[n]\n\t"
        "buffer_load_dwordx4 %[vo], %[srd], %[so] offen nt lds\n\t"
        "s_add_u32 m0, m0, %c[ls]\n\t"
        "s_add_u32 %[so], %[so], %[gs]\n\t"
        ".endr"
        : [so] "=&s"(so)
        : [l] "s"(lds0), [s0] "s"(soff0), [n] "i"(N), [vo] "v"(voff), [srd] "s"(srd), [ls] "i"(LSTEP), [gs] "s"(gstep)
        : "memory", "scc");
}
#define WAIT_VM(n) asm volatile("s_waitcnt vmcnt(%0)" ::"i"(n) : "memory")
#define LD_HEAD "s_nop 0\n\t"
__device__ __forceinline__ unsigned opq(unsigned v) { asm("" : "+v"(v)); return v; }
__device__ __forceinline__ void pl1(v4u srd, unsigned vo, v4u& a) {
    asm volatile("buffer_load_dwordx4 %0, %1, %2, 0 offen sc1\n\ts_waitcnt vmcnt(0)" : "=&v"(a) : "v"(vo), "s"(srd) : "memory");
}
__device__ __forceinline__ void pl4(v4u srd, unsigned vo, unsigned s1, unsigned s2, unsigned s3, v4u& a, v4u& b, v4u& c, v4u& d) {
    asm volatile("buffer_load_dwordx4 %0, %4, %5, 0 offen sc1\n\tbuffer_load_dwordx4 %1, %4, %5, %6 offen sc1\n\t"
                 "buffer_load_dwordx4 %2, %4, %5, %7 offen sc1\n\tbuffer_load_dwordx4 %3, %4, %5, %8 offen sc1\n\ts_waitcnt vmcnt(0)"
                 : "=&v"(a), "=&v"(b), "=&v"(c), "=&v"(d) : "v"(vo), "s"(srd), "s"(s1), "s"(s2), "s"(s3) : "memory");
}
__device__ __forceinline__ bool any_lane(bool c) { return __builtin_amdgcn_ballot_w64(c) != 0; }
__device__ __forceinline__ unsigned tag_bad(v4u v, unsigned tlo, unsigned thi) { return (v.y ^ tlo) | (v.w ^ thi); }
#define ENDPGM() __builtin_amdgcn_endpgm()
#define SCHED() __builtin_amdgcn_sched_barrier(0)
#define BARRIER() asm volatile("s_waitcnt lgkmcnt(0)\n\ts_barrier" ::: "memory")
#define CLOB() asm volatile("" ::: "memory")
typedef __amdgpu_buffer_rsrc_t rsrc_t;
__device__ __forceinline__ v4u ld16(rsrc_t r, int off) { return __builtin_amdgcn_raw_buffer_load_b128(r, off, 0, 16); }
__device__ __forceinline__ v2u ld8(rsrc_t r, int off) { return __builtin_amdgcn_raw_buffer_load_b64(r, off, 0, 16); }
__device__ __forceinline__ void st16(rsrc_t r, int off, v4u v) { __builtin_amdgcn_raw_buffer_store_b128(v, r, off, 0, 16); }
__device__ __forceinline__ void st8(rsrc_t r, int off, v2u v) { __builtin_amdgcn_raw_buffer_store_b64(v, r, off, 0, 16); }
__device__ __forceinline__ float f_of(unsigned u) { return __builtin_bit_cast(float, u); }
__device__ __forceinline__ unsigned u_of(float f) { return __builtin_bit_cast(unsigned, f); }
__device__ __forceinline__ float rbf(float x) { return (float)(__bf16)x; }
__device__ __forceinline__ unsigned pk2(float a, float b) { return __builtin_bit_cast(unsigned, __builtin_convertvector(v2f{a, b}, v2bf)); }
__device__ __forceinline__ float lo16(unsigned u) { return f_of(u << 16); }
__device__ __forceinline__ float hi16(unsigned u) { return f_of(u & 0xffff0000u); }
template <int CTRL> __device__ __forceinline__ float dpp(float x) {
    return __builtin_bit_cast(float, __builtin_amdgcn_mov_dpp(__builtin_bit_cast(int, x), CTRL, 0xf, 0xf, true));
}
constexpr int X1 = 0xB1, X2 = 0x4E, ROR4 = 0x124, ROR8 = 0x128;
__device__ __forceinline__ float add16(float a, float b) { auto s = __builtin_amdgcn_permlane16_swap(u_of(a), u_of(b), false, false); return f_of(s[0]) + f_of(s[1]); }
__device__ __forceinline__ float add32(float a, float b) { auto s = __builtin_amdgcn_permlane32_swap(u_of(a), u_of(b), false, false); return f_of(s[0]) + f_of(s[1]); }
__device__ __forceinline__ float max16(float a) { auto s = __builtin_amdgcn_permlane16_swap(u_of(a), u_of(a), false, false); return fmaxf(f_of(s[0]), f_of(s[1])); }
__device__ __forceinline__ float max32(float a) { auto s = __builtin_amdgcn_permlane32_swap(u_of(a), u_of(a), false, false); return fmaxf(f_of(s[0]), f_of(s[1])); }
__device__ __forceinline__ float wave_sum(float v) {
    v += dpp<X1>(v); v += dpp<X2>(v); v += dpp<ROR4>(v); v += dpp<ROR8>(v); v = add16(v, v); return add32(v, v);
}
__device__ __forceinline__ float ex2(float x) { return __builtin_amdgcn_exp2f(x); }
__device__ __forceinline__ float fdiv(float n, float d) {
    const float r = __builtin_amdgcn_rcpf(d), q = n * r;
    return fmaf(fmaf(-q, d, n), r, q);
}
__device__ __forceinline__ float sigm(float x) { return __builtin_amdgcn_rcpf(1.f + ex2(-x * LOG2E)); }
__device__ __forceinline__ v8bf tr_frag(unsigned a0, unsigned a1) {
    const v4s t0 = __builtin_amdgcn_ds_read_tr16_b64_v4i16((LAS v4s*)a0), t1 = __builtin_amdgcn_ds_read_tr16_b64_v4i16((LAS v4s*)a1);
    return __builtin_bit_cast(v8bf, __builtin_shufflevector(t0, t1, 0, 1, 2, 3, 4, 5, 6, 7));
}
__device__ __forceinline__ v8bf lds_frag(unsigned a) { return *(LAS v8bf*)a; }
__device__ __forceinline__ v4f mfma(v8bf a, v8bf b, v4f c) { return __builtin_amdgcn_mfma_f32_16x16x32_bf16(a, b, c, 0, 0, 0); }
__device__ __forceinline__ bool tag_ok(v4u v, unsigned tlo, unsigned thi) { return v.y == tlo && v.w == thi; }
__device__ __forceinline__ unsigned umax4(v4u v) { return max(max(v.x, v.y), max(v.z, v.w)); }
__device__ __forceinline__ u64 rt() { return __builtin_amdgcn_s_memrealtime(); }

#define STAMP(k) do {} while (0)
#define STAMP_OUT() do {} while (0)

__device__ __forceinline__ unsigned tile_rd_addr(unsigned base, int w, int lane) {
    const int G = lane >> 4, q = (lane & 15) >> 2, p = lane & 3;
    return base + (8 * G + q) * 128 + (((2 * w + (p >> 1)) ^ (2 * ((q >> 1) | ((G & 1) << 1)))) << 4) + (p & 1) * 8;
}
__device__ __forceinline__ unsigned tile_dma_voff(int pitch, int w, int lane) {
    const int r8 = lane >> 3, p = lane & 7;
    return r8 * pitch + ((p ^ (2 * (((r8 >> 1) & 1) | ((w & 1) << 1)))) << 4);
}
__device__ __forceinline__ void silu_to_lds(unsigned cw, int tid) {
    const float a = lo16(cw), b = hi16(cw);
    *(LAS unsigned*)(L_SC + (tid & 127) * 4) = pk2(a * sigm(a), b * sigm(b));
}
__device__ __forceinline__ void mod_tile(unsigned base, rsrc_t wsr, int cb, int kh, int w, int lane, unsigned tlo, unsigned thi) {
    const unsigned ra = opq(tile_rd_addr(base, w, lane)), rb = opq(L_SC + 16 * (lane >> 4));
    v8bf wf[8], af[8];
#pragma unroll
    for (int t = 0; t < 8; ++t) { wf[t] = tr_frag(ra + t * 4096, ra + t * 4096 + 512); af[t] = lds_frag(rb + 64 * t); }
    SCHED();
    v4f d = {0.f, 0.f, 0.f, 0.f};
#pragma unroll
    for (int t = 0; t < 8; ++t) d = mfma(wf[t], af[t], d);
    const int e = lane & 15;
    if (e < 2) st16(wsr, WS_MOD + kh * MOD_HALF + (32 * cb + 8 * w + 2 * (lane >> 4) + e) * 16, v4u{u_of(e ? d.z : d.x), tlo, u_of(e ? d.w : d.y), thi});
}

#define TAG_FROM(ev) const u64 T_ = (((u64)(unsigned)__builtin_amdgcn_readfirstlane((ev).y) << 32) | (unsigned)__builtin_amdgcn_readfirstlane((ev).x)) + 1
__device__ __forceinline__ v2u tag_from_lds() {
    const v2u t = *(LAS v2u*)L_TAG;
    return v2u{(unsigned)__builtin_amdgcn_readfirstlane(t.x), (unsigned)__builtin_amdgcn_readfirstlane(t.y)};
}

__global__ void __launch_bounds__(512, 2) __attribute__((amdgpu_waves_per_eu(2, 2))) fused_block(Params p) {
    const int b = blockIdx.x, xcd = b & 7, ib = b >> 3, tid = threadIdx.x, lane = tid & 63;
    const int w = __builtin_amdgcn_readfirstlane(tid >> 6);
    const int G = lane >> 4, c16 = lane & 15;
    STAMP(0);
    const u64 t_start = rt();
    const rsrc_t wsr = __builtin_amdgcn_make_buffer_rsrc(p.ws, 0, WS_END, 0x20000);
    const v4u wsd = mk_srd(p.ws, WS_END);
    if (ib < 16) {
        const int g = xcd, s = ib;
        if (w < 4) {
            const size_t hb = ((size_t)g * SK + (SK - WIN)) * 128;
            const unsigned so0 = (KV_INTERLEAVE ? s * 4096 : s * 65536) + w * 1024, gs = KV_INTERLEAVE ? 65536 : 4096;
            dma_seq<16, 4096>(mk_srd(p.kc + hb, 1 << 20), G * 256 + ((c16 ^ (4 * w + G)) << 4), so0, gs, L_T0 + w * 1024);
            dma_seq<16, 4096>(mk_srd(p.vc + hb, 1 << 20), G * 256 + ((c16 ^ (2 * G | ((w & 2) << 2))) << 4), so0, gs, L_T1 + w * 1024);
            STAMP(1);
            WAIT_VM(16);
        } else if (w == 7) {
            const v4u s4 = {SENT, SENT, SENT, SENT};
            st16(wsr, WS_H3 + ((4 * g + (lane >> 5)) * 16 + s) * 512 + (lane & 31) * 16, s4);
            st16(wsr, WS_H3 + ((4 * g + 2 + (lane >> 5)) * 16 + s) * 512 + (lane & 31) * 16, s4);
            if (lane < 4) st8(wsr, WS_ML + ((4 * g + lane) * 16 + s) * 8, v2u{SENT, SENT});
            if (lane < 16) st16(wsr, WS_H4 + (2 * g + (s >> 3)) * 2048 + (s & 7) * 256 + lane * 16, s4);
            v2u ev;
            asm volatile("buffer_load_dwordx2 %0, off, %1, 0 sc1\n\ts_waitcnt vmcnt(0)" : "=&v"(ev) : "s"(wsd) : "memory");
            TAG_FROM(ev);
            STAMP(1);
            if (lane == 0) { st16(wsr, WS_FLGA + (16 * g + s) * 16, v4u{0u, (unsigned)T_, 0u, (unsigned)(T_ >> 32)}); *(LAS v2u*)L_TAG = v2u{(unsigned)T_, (unsigned)(T_ >> 32)}; }
        }
        STAMP(2);
        BARRIER();
        v8bf kf[2][4];
#pragma unroll
        for (int t = 0; t < 4; ++t) {
            const unsigned a = L_T0 + (32 * w + c16) * 256 + (((4 * t + G) ^ c16) << 4);
            kf[0][t] = lds_frag(a); kf[1][t] = lds_frag(a + 4096);
        }
        const int ks = 2 * g + (s >> 3), j = s & 7;
        const unsigned qra = opq(L_Q + (lane & 3) * 320 + 16 * G), pra = opq(L_P + (lane & 3) * 576 + 16 * G), sra = opq(L_ST + (lane & 3) * 64);
        const unsigned vra = opq(L_T1 + (8 * G + (c16 >> 2)) * 256 + (((2 * w + ((lane & 3) >> 1)) ^ (2 * (c16 >> 2) | ((G & 1) << 3))) << 4) + (lane & 1) * 8);
        const unsigned pwa = opq(L_P + (lane & 3) * 576 + (32 * w + ((lane & 4) << 2) + 4 * G + ((lane & 8) >> 2)) * 2);
        const unsigned h3st = opq(WS_H3 + ((4 * g + (lane & 3)) * 16 + s) * 512 + (16 * w + 4 * G) * 4);
        const unsigned wovo = opq(tile_dma_voff(1024, w, lane));
        if (w < 4) {
            const v2u tg = tag_from_lds();
            const unsigned qoff = lane < 32 ? WS_Q + (32 * (4 * g + w) + lane) * 16 : WS_FLGA + (16 * g + c16) * 16;
            const unsigned qwa = opq(L_Q + w * 320 + (lane & 31) * 8);
            WAIT_VM(Q_POLL_VM);
            STAMP(11);
            v4u qv;
#pragma unroll 1
            for (int it = 0; it < SPIN_CAP; ++it) { pl1(wsd, qoff, qv); if (!any_lane(tag_bad(qv, tg.x, tg.y) != 0)) break; }
            if (lane < 32) *(LAS v2u*)qwa = v2u{qv.x, qv.z};
        }
        STAMP(3);
        BARRIER();
        STAMP(4);
        v8bf qf[4], vf[8];
#pragma unroll
        for (int t = 0; t < 4; ++t) qf[t] = lds_frag(qra + 64 * t);
        if (w < 4) dma_seq<8, 4096>(mk_srd(p.wo + (size_t)ks * 256 * 512 + 64 * j, 256 * 1024), wovo, w * 8192, 32768, L_T0 + w * 1024);
#pragma unroll
        for (int t = 0; t < 8; ++t) vf[t] = tr_frag(vra + t * 8192, vra + t * 8192 + 1024);
        SCHED();
        v4f d0 = {0.f, 0.f, 0.f, 0.f}, d1 = d0;
#pragma unroll
        for (int t = 0; t < 4; ++t) { d0 = mfma(kf[0][t], qf[t], d0); d1 = mfma(kf[1][t], qf[t], d1); }
        {
            const bool tau = lane & 4, pr = lane & 8;
            const float a0 = pr ? d0.z : d0.x, a1 = pr ? d0.w : d0.y, b0 = pr ? d1.z : d1.x, b1 = pr ? d1.w : d1.y;
            const float s0 = rbf(rbf(tau ? b0 : a0) * 0.08838834764831845f), s1 = rbf(rbf(tau ? b1 : a1) * 0.08838834764831845f);
            float m = fmaxf(s0, s1);
            m = fmaxf(m, dpp<ROR4>(m)); m = fmaxf(m, dpp<ROR8>(m)); m = max16(m); m = max32(m);
            const float p0 = ex2((s0 - m) * LOG2E), p1 = ex2((s1 - m) * LOG2E);
            *(LAS unsigned*)pwa = pk2(p0, p1);
            float l = p0 + p1;
            l += dpp<ROR4>(l); l += dpp<ROR8>(l); l = add16(l, l); l = add32(l, l);
            if (lane < 4) { *(LAS float*)(sra + w * 4) = m; *(LAS float*)(sra + 32 + w * 4) = l; }
        }
        STAMP(5);
        BARRIER();
        {
            v8bf pf[8];
            const v4f ma = *(const LAS v4f*)sra, mb = *(const LAS v4f*)(sra + 16);
#pragma unroll
            for (int t = 0; t < 8; ++t) pf[t] = lds_frag(pra + 64 * t);
            const v4f la = *(const LAS v4f*)(sra + 32), lb = *(const LAS v4f*)(sra + 48);
            SCHED();
            v4f dv[8];
#pragma unroll
            for (int t = 0; t < 8; ++t) dv[t] = mfma(vf[t], pf[t], v4f{0.f, 0.f, 0.f, 0.f});
            const float M = fmaxf(fmaxf(fmaxf(ma.x, ma.y), fmaxf(ma.z, ma.w)), fmaxf(fmaxf(mb.x, mb.y), fmaxf(mb.z, mb.w)));
            float f[8];
#pragma unroll
            for (int t = 0; t < 4; ++t) { f[t] = ex2((ma[t] - M) * LOG2E); f[4 + t] = ex2((mb[t] - M) * LOG2E); }
            v4f o = dv[0] * f[0];
#pragma unroll
            for (int t = 1; t < 8; ++t) o += dv[t] * f[t];
            if (c16 < 4) st16(wsr, h3st, __builtin_bit_cast(v4u, o));
            if (w == 4) {
                float L = 0.f;
#pragma unroll
                for (int t = 0; t < 4; ++t) L += f[t] * la[t] + f[4 + t] * lb[t];
                if (lane < 4) st8(wsr, WS_ML + ((4 * g + lane) * 16 + s) * 8, v2u{u_of(M), u_of(L)});
            }
        }
        STAMP(6);
        const int hd = 2 * ks + (w >> 2), s8 = lane >> 3;
        const unsigned o3 = WS_H3 + (hd * 16 + s8) * 512 + (w & 3) * 128 + (lane & 7) * 16, oml = WS_ML + (hd * 16 + s8) * 8;
        const unsigned owa = opq(L_O + ((w >> 2) * 128 + (w & 3) * 32 + 4 * (lane & 7) + G) * 2), ora = opq(L_O + 16 * G);
        const unsigned h4st = opq(WS_H4 + ks * 2048 + (64 * j + 16 * w + 4 * G) * 4);
        if (w < 4) WAIT_VM(1);
        BARRIER();
        STAMP(7);
        v8bf wf[8];
        {
            const unsigned ra = opq(tile_rd_addr(L_T0, w & 3, lane));
#pragma unroll
            for (int t = 0; t < 8; ++t) wf[t] = tr_frag(ra + t * 4096, ra + t * 4096 + 512);
        }
        v4u a0, a1; v2u m0, m1;
#pragma unroll 1
        for (int it = 0; it < SPIN_CAP; ++it) {
            asm volatile("buffer_load_dwordx2 %2, %5, %6, 0 offen sc1\n\tbuffer_load_dwordx2 %3, %5, %6, 0 offen offset:64 sc1\n\t"
                         "buffer_load_dwordx4 %0, %4, %6, 0 offen sc1\n\tbuffer_load_dwordx4 %1, %4, %6, %7 offen sc1\n\ts_waitcnt vmcnt(0)"
                         : "=&v"(a0), "=&v"(a1), "=&v"(m0), "=&v"(m1) : "v"(o3), "v"(oml), "s"(wsd), "s"(8 * 512) : "memory");
            const unsigned mx = max(max(umax4(a0), umax4(a1)), max(max(m0.x, m0.y), max(m1.x, m1.y)));
            if (!any_lane(mx == SENT)) break;
        }
        STAMP(8);
        {
            float M = fmaxf(f_of(m0.x), f_of(m1.x));
            M = fmaxf(M, dpp<ROR8>(M)); M = max16(M); M = max32(M);
            const float w0 = ex2((f_of(m0.x) - M) * LOG2E), w1 = ex2((f_of(m1.x) - M) * LOG2E);
            float L = w0 * f_of(m0.y) + w1 * f_of(m1.y);
            const v4f v = __builtin_bit_cast(v4f, a0) * w0 + __builtin_bit_cast(v4f, a1) * w1;
            L += dpp<ROR8>(L); L = add16(L, L); L = add32(L, L);
            float z = add16(add32(v.x, v.z), add32(v.y, v.w));
            z += dpp<ROR8>(z);
            *(LAS u16*)owa = (u16)pk2(fdiv(z, L), 0.f);
        }
        BARRIER();
        STAMP(9);
        if (w < 4) {
            v8bf of[8];
#pragma unroll
            for (int t = 0; t < 8; ++t) of[t] = lds_frag(ora + 64 * t);
            SCHED();
            v4f d = {0.f, 0.f, 0.f, 0.f};
#pragma unroll
            for (int t = 0; t < 8; ++t) d = mfma(wf[t], of[t], d);
            if (c16 == 0) st16(wsr, h4st, __builtin_bit_cast(v4u, d));
        }
        if (w == 0) { const v2u tq_ = tag_from_lds(); const u64 tn_ = rt(); if (lane == 0) { st16(wsr, WS_EXP + b * 16, v4u{(unsigned)t_start, tq_.x, 0u, tq_.y}); st16(wsr, WS_EXP + 4096 + b * 16, v4u{(unsigned)tn_, tq_.x, 0u, tq_.y}); } }
        ENDPGM();
    }
    if (ib < 24) {
        const int g = xcd, j = ib - 16, mt = xcd * 8 + j, cb = 16 + (mt >> 1), kh = mt & 1;
        if (w < 4) {
            dma_seq<16, 4096>(mk_srd(p.wq + 512 * g + 64 * j, 512 * 8192), tile_dma_voff(8192, w, lane), w * 65536, 4 * 65536, L_T0 + w * 1024);
            STAMP(1);
            WAIT_VM(0);
            STAMP(2);
            BARRIER();
            v8bf wf[16];
            const unsigned ra = opq(tile_rd_addr(L_T0, w, lane)), rb = opq(L_ACT + 16 * G);
#pragma unroll
            for (int t = 0; t < 16; ++t) wf[t] = tr_frag(ra + t * 4096, ra + t * 4096 + 512);
            const v2u tg = tag_from_lds();
            const unsigned qst = opq(WS_Q + (128 * g + 16 * j + 4 * w + G) * 16), mvo = opq(tile_dma_voff(6144, w, lane));
            BARRIER();
            STAMP(4);
            v8bf hf[16];
#pragma unroll
            for (int t = 0; t < 16; ++t) hf[t] = lds_frag(rb + 64 * t);
            SCHED();
            v4f d = {0.f, 0.f, 0.f, 0.f};
#pragma unroll
            for (int t = 0; t < 16; ++t) d = mfma(wf[t], hf[t], d);
            if (c16 == 0) st16(wsr, qst, v4u{pk2(d.x, d.y), tg.x, pk2(d.z, d.w), tg.y});
            STAMP(5);
            dma_seq<8, 4096>(mk_srd(p.wm + (size_t)kh * 256 * 3072 + 64 * cb, 256 * 6144), mvo, w * 8 * 6144, 32 * 6144, L_T1 + w * 1024);
            WAIT_VM(0);
            BARRIER();
            STAMP(6);
            mod_tile(L_T1, wsr, cb, kh, w, lane, tg.x, tg.y);
            if (w == 0) { const v2u tq_ = tag_from_lds(); const u64 tn_ = rt(); if (lane == 0) { st16(wsr, WS_EXP + b * 16, v4u{(unsigned)t_start, tq_.x, 0u, tq_.y}); st16(wsr, WS_EXP + 4096 + b * 16, v4u{(unsigned)tn_, tq_.x, 0u, tq_.y}); } }
            ENDPGM();
        }
        const int hp = 64 * (w & 3) + lane;
        v2u ev; v4u x8; unsigned x2w, cw;
        asm volatile("buffer_load_dwordx2 %0, off, %4, 0 sc1\n\tglobal_load_dwordx4 %1, %5, %8\n\tglobal_load_dword %2, %6, %8\n\tglobal_load_dword %3, %7, %9\n\ts_waitcnt vmcnt(0)"
                     : "=&v"(ev), "=&v"(x8), "=&v"(x2w), "=&v"(cw)
                     : "s"(wsd), "v"(lane * 16), "v"(hp * 4), "v"((128 * kh + (tid & 127)) * 4), "s"(p.x), "s"(p.c) : "memory");
        TAG_FROM(ev);
        const unsigned tlo = (unsigned)T_, thi = (unsigned)(T_ >> 32);
        *(LAS v2u*)L_TAG = v2u{tlo, thi};
        STAMP(1);
        silu_to_lds(cw, tid);
        float xs[8];
#pragma unroll
        for (int k = 0; k < 4; ++k) { xs[2 * k] = lo16(x8[k]); xs[2 * k + 1] = hi16(x8[k]); }
        float sum = 0.f;
#pragma unroll
        for (int k = 0; k < 8; ++k) sum += xs[k];
        const float mean = wave_sum(sum) * (1.f / 512.f);
        float sq = 0.f;
#pragma unroll
        for (int k = 0; k < 8; ++k) sq = fmaf(xs[k] - mean, xs[k] - mean, sq);
        const float var = rbf(wave_sum(sq) * (1.f / 512.f)), mr = rbf(mean), den = rbf(__builtin_amdgcn_sqrtf(rbf(var + 1e-5f)));
        const float ln0 = rbf(fdiv(rbf(lo16(x2w) - mr), den)), ln1 = rbf(fdiv(rbf(hi16(x2w) - mr), den));
        const unsigned hwa = opq(L_ACT + hp * 4);
        BARRIER();
        STAMP(2);
        v4u sa0, sa1, ca0, ca1;
#pragma unroll 1
        for (int it = 0; it < SPIN_CAP; ++it) {
            pl4(wsd, WS_MOD + hp * 16, MOD_HALF, 4096, 4096 + MOD_HALF, sa0, sa1, ca0, ca1);
            if (!any_lane((tag_bad(sa0, tlo, thi) | tag_bad(sa1, tlo, thi) | tag_bad(ca0, tlo, thi) | tag_bad(ca1, tlo, thi)) != 0)) break;
        }
        STAMP(3);
        const float h0 = rbf(ln0 * rbf(1.f + rbf(f_of(ca0.x) + f_of(ca1.x)))) + rbf(f_of(sa0.x) + f_of(sa1.x));
        const float h1 = rbf(ln1 * rbf(1.f + rbf(f_of(ca0.z) + f_of(ca1.z)))) + rbf(f_of(sa0.z) + f_of(sa1.z));
        *(LAS unsigned*)hwa = pk2(h0, h1);
        BARRIER();
        STAMP(4);
        STAMP_OUT();
        BARRIER();
        ENDPGM();
    }
    {
        const int u = ib - 24;
        const int f = xcd < 4 ? 8 * xcd + u : (u < 6 ? 32 + 6 * (xcd - 4) + u : -1);
        if (f < 0) ENDPGM();
        const u64 t_in = F_DELAY ? rt() : 0;
        const int mt = xcd * 4 + u, cb = mt >> 1, kh = mt & 1;
        if (w < 4) {
            if (u < 4) {
                dma_seq<8, 4096>(mk_srd(p.wm + (size_t)kh * 256 * 3072 + 64 * cb, 256 * 6144), tile_dma_voff(6144, w, lane), w * 8 * 6144, 32 * 6144, L_T2 + w * 1024);
                STAMP(1);
                WAIT_VM(0);
            }
        } else {
            v2u ev; unsigned cw;
            asm volatile("buffer_load_dwordx2 %0, off, %2, 0 sc1\n\tglobal_load_dword %1, %3, %4\n\ts_waitcnt vmcnt(0)"
                         : "=&v"(ev), "=&v"(cw) : "s"(wsd), "v"((128 * kh + (tid & 127)) * 4), "s"(p.c) : "memory");
            TAG_FROM(ev);
            *(LAS v2u*)L_TAG = v2u{(unsigned)T_, (unsigned)(T_ >> 32)};
            silu_to_lds(cw, tid);
            STAMP(1);
        }
        BARRIER();
        STAMP(2);
        const v2u tg = tag_from_lds();
        const unsigned tlo = tg.x, thi = tg.y;
        if (w < 4) { if (u < 4) mod_tile(L_T2, wsr, cb, kh, w, lane, tlo, thi); }
        else if (w == 7) {
            const v4u s4 = {SENT, SENT, SENT, SENT};
            st16(wsr, WS_H6 + f * 2048 + lane * 16, s4);
            st16(wsr, WS_H6 + f * 2048 + 1024 + lane * 16, s4);
        }
        BARRIER();
        STAMP(3);
        if (F_DELAY) { while (rt() - t_in < F_DELAY) __builtin_amdgcn_s_sleep(2); }
        const int col = 64 * w + 4 * c16 + G, colr = 32 * f + 4 * (lane & 7) + G;
        if (w < 4) {
            const unsigned vo = (lane >> 2) * 3584, pz = (lane & 3) ^ (lane >> 5);
            dma_seq<8, 4096>(mk_srd(p.wg + 32 * f, 512 * 3584), vo + (pz << 4), w * 16 * 3584, 64 * 3584, L_T0 + w * 1024);
            dma_seq<8, 4096>(mk_srd(p.wu + 32 * f, 512 * 3584), vo + ((pz ^ 2) << 4), w * 16 * 3584, 64 * 3584, L_T0 + 32768 + w * 1024);
            dma_seq<8, 4096>(mk_srd(p.wd + (size_t)32 * f * 512, 32 * 1024), (lane ^ (2 * w)) << 4, w * 1024, 4096, L_T2 + w * 1024);
        } else if (w == 7) {
            WAIT_VM(0);
            if (lane == 0) st16(wsr, WS_FLGF + f * 16, v4u{0u, tlo, 0u, thi});
        }
        BARRIER();
        STAMP(4);
        const bool red = f < 16 && w == 7;
        float ga, sb, cb1, gb, xi;
        {
            const unsigned om = WS_MOD + (512 + (col >> 1)) * 16, of = WS_FLGA + (16 * w + c16) * 16;
            const unsigned og = WS_MOD + (1280 + ((colr & 511) >> 1)) * 16, off = WS_FLGF + (lane < 56 ? lane : 0) * 16;
            v4u g0, g1, s0, s1, c0, c1, fa, b0, b1, fb; unsigned xc;
#pragma unroll 1
            for (int it = 0; it < SPIN_CAP; ++it) {
                asm volatile("buffer_load_dwordx4 %0, %11, %15, 0 offen sc1\n\tbuffer_load_dwordx4 %1, %11, %15, %16 offen sc1\n\t"
                             "buffer_load_dwordx4 %2, %11, %15, %17 offen sc1\n\tbuffer_load_dwordx4 %3, %11, %15, %18 offen sc1\n\t"
                             "buffer_load_dwordx4 %4, %11, %15, %19 offen sc1\n\tbuffer_load_dwordx4 %5, %11, %15, %20 offen sc1\n\t"
                             "buffer_load_dwordx4 %6, %12, %15, 0 offen sc1\n\tbuffer_load_dwordx4 %7, %13, %15, 0 offen sc1\n\t"
                             "buffer_load_dwordx4 %8, %13, %15, %16 offen sc1\n\tbuffer_load_dwordx4 %9, %14, %15, 0 offen sc1\n\t"
                             "global_load_ushort %10, %21, %22\n\ts_waitcnt vmcnt(0)"
                             : "=&v"(g0), "=&v"(g1), "=&v"(s0), "=&v"(s1), "=&v"(c0), "=&v"(c1), "=&v"(fa), "=&v"(b0), "=&v"(b1), "=&v"(fb), "=&v"(xc)
                             : "v"(om), "v"(of), "v"(og), "v"(off), "s"(wsd), "s"(MOD_HALF), "s"(4096), "s"(4096 + MOD_HALF), "s"(8192), "s"(8192 + MOD_HALF),
                               "v"(col * 2), "s"(p.x) : "memory");
                const unsigned bad = tag_bad(g0, tlo, thi) | tag_bad(g1, tlo, thi) | tag_bad(s0, tlo, thi) | tag_bad(s1, tlo, thi) | tag_bad(c0, tlo, thi) |
                                     tag_bad(c1, tlo, thi) | tag_bad(fa, tlo, thi) | tag_bad(b0, tlo, thi) | tag_bad(b1, tlo, thi) | tag_bad(fb, tlo, thi);
                if (!any_lane(bad != 0)) break;
                __builtin_amdgcn_s_sleep(2);
            }
            const bool od = G & 1;
            ga = rbf(od ? f_of(g0.z) + f_of(g1.z) : f_of(g0.x) + f_of(g1.x));
            sb = rbf(od ? f_of(s0.z) + f_of(s1.z) : f_of(s0.x) + f_of(s1.x));
            cb1 = rbf(1.f + rbf(od ? f_of(c0.z) + f_of(c1.z) : f_of(c0.x) + f_of(c1.x)));
            gb = rbf(od ? f_of(b0.z) + f_of(b1.z) : f_of(b0.x) + f_of(b1.x));
            xi = lo16(xc);
        }
        STAMP(5);
        BARRIER();
        STAMP(6);
        v8bf gf[16], df[4];
        {
            const int q = c16 >> 2, pp = lane & 3;
            const unsigned ra = opq(L_T0 + (pp >> 1) * 32768 + (8 * G + q) * 64 + (((w & 3) ^ ((G & 1) | (pp & 2))) << 4) + (pp & 1) * 8);
#pragma unroll
            for (int t = 0; t < 16; ++t) gf[t] = tr_frag(ra + t * 2048, ra + t * 2048 + 256);
#pragma unroll
            for (int t = 0; t < 4; ++t) {
                const unsigned rd = opq(L_T2 + (8 * G + q) * 1024 + ((8 * w + ((2 * t) ^ (2 * q)) + (pp >> 1)) << 4) + (pp & 1) * 8);
                df[t] = tr_frag(rd, rd + 4096);
            }
        }
        const unsigned x2wa = opq(L_X2 + col * 4), hwa = opq(L_ACT + col * 2), hra = opq(L_ACT + 16 * G), awa = opq(L_A2 + (8 * w + 4 * (G & 1) + (lane & 3)) * 2);
        const unsigned ara = opq(L_A2 + 16 * G), h6st = opq(WS_H6 + f * 2048 + (64 * w + 4 * G) * 4), rwa = opq(L_RED + w * 4), rra = opq(L_RED);
        v4u a0, a1, a2, a3;
#pragma unroll 1
        for (int it = 0; it < SPIN_CAP; ++it) {
            pl4(wsd, WS_H4 + G * 2048 + (64 * w + 4 * c16) * 4, 4 * 2048, 8 * 2048, 12 * 2048, a0, a1, a2, a3);
            const unsigned mx = max(max(umax4(a0), umax4(a1)), max(umax4(a2), umax4(a3)));
            if (!any_lane(mx == SENT)) break;
        }
        STAMP(7);
        const v4f sv = (__builtin_bit_cast(v4f, a0) + __builtin_bit_cast(v4f, a1)) + (__builtin_bit_cast(v4f, a2) + __builtin_bit_cast(v4f, a3));
        const float ao = rbf(add16(add32(sv.x, sv.z), add32(sv.y, sv.w)));
        const float x2 = rbf(xi + rbf(ga * ao));
        {
            const float s1 = wave_sum(x2), s2 = wave_sum(x2 * x2);
            *(LAS float*)rwa = s1; *(LAS float*)(rwa + 32) = s2;
            *(LAS float*)x2wa = x2;
        }
        BARRIER();
        STAMP(8);
        {
            const LAS v4f* rp = (const LAS v4f*)rra;
            const v4f r0 = rp[0] + rp[1], r1 = rp[2] + rp[3];
            const float mean = ((r0.x + r0.y) + (r0.z + r0.w)) * (1.f / 512.f), ex = ((r1.x + r1.y) + (r1.z + r1.w)) * (1.f / 512.f);
            const float var = rbf(fmaxf(fmaf(-mean, mean, ex), 0.f)), den = rbf(__builtin_amdgcn_sqrtf(rbf(var + 1e-5f)));
            const float ln = rbf(fdiv(rbf(x2 - rbf(mean)), den));
            *(LAS u16*)hwa = (u16)pk2(rbf(ln * cb1) + sb, 0.f);
        }
        BARRIER();
        STAMP(9);
        if (w < 4) {
            v8bf hf[16];
#pragma unroll
            for (int t = 0; t < 16; ++t) hf[t] = lds_frag(hra + 64 * t);
            SCHED();
            v4f d = {0.f, 0.f, 0.f, 0.f};
#pragma unroll
            for (int t = 0; t < 16; ++t) d = mfma(gf[t], hf[t], d);
            const float v = (lane & 2) ? ((lane & 1) ? d.w : d.z) : ((lane & 1) ? d.y : d.x);
            const auto sw = __builtin_amdgcn_permlane32_swap(u_of(v), u_of(v), false, false);
            const float gt = rbf(f_of(sw[0])), up = rbf(f_of(sw[1]));
            const float ge = rbf(gt * sigm(1.5957691216057308f * fmaf(0.044715f * gt * gt, gt, gt)));
            *(LAS u16*)awa = (u16)pk2(ge * up, 0.f);
        }
        BARRIER();
        STAMP(10);
        {
            const v8bf af = lds_frag(ara);
            v4f d[4];
#pragma unroll
            for (int t = 0; t < 4; ++t) d[t] = mfma(df[t], af, v4f{0.f, 0.f, 0.f, 0.f});
            if (c16 == 0) {
#pragma unroll
                for (int t = 0; t < 4; ++t) st16(wsr, h6st + 64 * t, __builtin_bit_cast(v4u, d[t]));
            }
        }
        if (w == 0) { const v2u tq_ = tag_from_lds(); const u64 tn_ = rt(); if (lane == 0) { st16(wsr, WS_EXP + b * 16, v4u{(unsigned)t_start, tq_.x, 0u, tq_.y}); st16(wsr, WS_EXP + 4096 + b * 16, v4u{(unsigned)tn_, tq_.x, 0u, tq_.y}); } }
        if (red) {
            const float xr = *(LAS float*)(L_X2 + colr * 4);
            const unsigned o6 = WS_H6 + (lane >> 3) * 2048 + (32 * f + 4 * (lane & 7)) * 4;
            v4u r0, r1, r2, r3, r4, r5, r6;
#pragma unroll 1
            for (int it = 0; it < SPIN_CAP; ++it) {
                asm volatile("buffer_load_dwordx4 %0, %7, %8, 0 offen sc1\n\tbuffer_load_dwordx4 %1, %7, %8, %9 offen sc1\n\t"
                             "buffer_load_dwordx4 %2, %7, %8, %10 offen sc1\n\tbuffer_load_dwordx4 %3, %7, %8, %11 offen sc1\n\t"
                             "buffer_load_dwordx4 %4, %7, %8, %12 offen sc1\n\tbuffer_load_dwordx4 %5, %7, %8, %13 offen sc1\n\t"
                             "buffer_load_dwordx4 %6, %7, %8, %14 offen sc1\n\ts_waitcnt vmcnt(0)"
                             : "=&v"(r0), "=&v"(r1), "=&v"(r2), "=&v"(r3), "=&v"(r4), "=&v"(r5), "=&v"(r6)
                             : "v"(o6), "s"(wsd), "s"(16384), "s"(2 * 16384), "s"(3 * 16384), "s"(4 * 16384), "s"(5 * 16384), "s"(6 * 16384) : "memory");
                const unsigned mx = max(max(max(umax4(r0), umax4(r1)), max(umax4(r2), umax4(r3))), max(max(umax4(r4), umax4(r5)), umax4(r6)));
                if (!any_lane(mx == SENT)) break;
            }
            STAMP(12);
            const v4f v = ((__builtin_bit_cast(v4f, r0) + __builtin_bit_cast(v4f, r1)) + (__builtin_bit_cast(v4f, r2) + __builtin_bit_cast(v4f, r3))) +
                          ((__builtin_bit_cast(v4f, r4) + __builtin_bit_cast(v4f, r5)) + __builtin_bit_cast(v4f, r6));
            float z = add16(add32(v.x, v.z), add32(v.y, v.w));
            z += dpp<ROR8>(z);
            if (!(lane & 8)) p.out[colr] = (u16)pk2(xr + rbf(gb * rbf(z)), 0.f);
            if (f == 0 && lane == 0) st8(wsr, WS_E, v2u{tlo, thi});
            if (f == 0) {
                unsigned X = 0;
                const unsigned t0 = (unsigned)t_start;
                if (0 == 1) X = (unsigned)rt() - t0;
                if (0 >= 2) {
                    int mn = 1 << 30, mx = -(1 << 30);
                    for (int k = 0; k < 4; ++k) {
                        const int bb = k * 64 + lane; const bool val = !((bb >> 3) >= 30 && (bb & 7) >= 4);
                        const bool sel = 0 == 2 || 0 == 3 ? true : 0 == 4 ? (bb >> 3) < 16 : 0 == 5 ? ((bb >> 3) >= 16 && (bb >> 3) < 24) : true;
                        v4u e;
                        for (int it = 0; it < SPIN_CAP; ++it) { pl1(wsd, WS_EXP + (0 >= 4 ? 4096 : 0) + bb * 16, e); if (!any_lane(val && tag_bad(e, tlo, thi) != 0)) break; }
                        if (val && sel) { const int d = (int)(e.x - t0); mn = min(mn, d); mx = max(mx, d); }
                    }
                    for (int o = 32; o; o >>= 1) { mn = min(mn, __shfl_xor(mn, o)); mx = max(mx, __shfl_xor(mx, o)); }
                    X = 0 == 3 ? -mn : mx;
                    X = __builtin_amdgcn_readfirstlane(X);
                }
                while ((unsigned)rt() - t0 < 2000u + X) __builtin_amdgcn_s_sleep(1);
            }
        }
        STAMP_OUT();
    }
}

__global__ void null_k(unsigned char* ws, int m) {
    if (m == 5) { if (threadIdx.x == 0) *(volatile unsigned*)(ws + (2 << 20)) = (unsigned)rt(); }
    if (m == 6) { const unsigned t0 = *(volatile unsigned*)(ws + (2 << 20)); while ((unsigned)rt() - t0 < 4500u) __builtin_amdgcn_s_sleep(1); }
}
extern "C" void kernel_launch(void* const* d_in, const int* in_sizes, int n_in, void* d_out, int out_size, void* d_ws, size_t ws_size,
                              hipStream_t stream) {
    Params p;
    p.x = (const u16*)d_in[0]; p.kc = (const u16*)d_in[1]; p.vc = (const u16*)d_in[2]; p.wq = (const u16*)d_in[3]; p.wo = (const u16*)d_in[4];
    p.wg = (const u16*)d_in[6]; p.wu = (const u16*)d_in[7]; p.wd = (const u16*)d_in[8]; p.c = (const u16*)d_in[9]; p.wm = (const u16*)d_in[10];
    p.out = (u16*)d_out; p.ws = (unsigned char*)d_ws;
    (void)hipFuncSetAttribute((const void*)fused_block, hipFuncAttributeMaxDynamicSharedMemorySize, LDS_BYTES);
    hipLaunchKernelGGL(fused_block, dim3(256), dim3(512), LDS_BYTES, stream, p);
}
```

```cpp
#include <hip/hip_runtime.h>
#include <stdint.h>
#pragma clang diagnostic ignored "-Wint-to-pointer-cast"

#define LAS __attribute__((address_space(3)))
typedef unsigned short u16;
typedef unsigned long long u64;
typedef short v4s __attribute__((ext_vector_type(4)));
typedef __bf16 v8bf __attribute__((ext_vector_type(8)));
typedef __bf16 v2bf __attribute__((ext_vector_type(2)));
typedef float v2f __attribute__((ext_vector_type(2)));
typedef float v4f __attribute__((ext_vector_type(4)));
typedef unsigned v4u __attribute__((ext_vector_type(4)));
typedef unsigned v2u __attribute__((ext_vector_type(2)));

#define STAMPS 0
#define KV_INTERLEAVE 1
#define Q_POLL_VM 5
#define F_DELAY 0

constexpr int SK = 65536, WIN = 4096, FF = 1792;
constexpr int WS_E = 0, WS_FLGA = 4096, WS_FLGF = 8192, WS_MOD = 16384, MOD_HALF = 1536 * 16, WS_Q = WS_MOD + 2 * MOD_HALF,
              WS_H3 = WS_Q + 16384, WS_ML = WS_H3 + 32 * 16 * 512, WS_H4 = WS_ML + 4096, WS_H6 = WS_H4 + 16 * 2048,
              WS_EXP = WS_H6 + 56 * 2048, WS_END = WS_EXP + 3 * 4096, WS_DBG = 1 << 20;
constexpr int L_T0 = 0, L_T1 = 65536, L_T2 = 98304, L_SM = 131072;
constexpr int L_Q = L_SM, L_P = L_Q + 4 * 320, L_ST = L_P + 4 * 576, L_O = L_ST + 256;
constexpr int L_ACT = L_SM, L_X2 = L_ACT + 1024, L_RED = L_X2 + 2048, L_A2 = L_RED + 128, L_SC = L_A2 + 64;
constexpr int L_TAG = L_SM + 5104, LDS_BYTES = L_SM + 5120;
constexpr unsigned SENT = 0xFFFFFFFFu;
constexpr int SPIN_CAP = 1 << 16;
constexpr float LOG2E = 1.4426950408889634f;

struct Params { const u16 *x, *kc, *vc, *wq, *wo, *wg, *wu, *wd, *c, *wm; u16* out; unsigned char* ws; };

__device__ __forceinline__ v4u mk_srd(const void* p, unsigned bytes) {
    const u64 a = (u64)p;
    return v4u{(unsigned)a, (unsigned)(a >> 32) & 0xffffu, bytes, 0x20000u};
}
template <int N, int LSTEP>
__device__ __forceinline__ void dma_seq(v4u srd, unsigned voff, unsigned soff0, unsigned gstep, unsigned lds0) {
    unsigned so;
    asm volatile(
        "s_nop 4\n\t"
        "s_mov_b32 m0, %[l]\n\t"
        "s_mov_b32 %[so], %[s0]\n\t"
        ".rept %c[n]\n\t"
        "buffer_load_dwordx4 %[vo], %[srd], %[so] offen nt lds\n\t"
        "s_add_u32 m0, m0, %c[ls]\n\t"
        "s_add_u32 %[so], %[so], %[gs]\n\t"
        ".endr"
        : [so] "=&s"(so)
        : [l] "s"(lds0), [s0] "s"(soff0), [n] "i"(N), [vo] "v"(voff), [srd] "s"(srd), [ls] "i"(LSTEP), [gs] "s"(gstep)
        : "memory", "scc");
}
#define WAIT_VM(n) asm volatile("s_waitcnt vmcnt(%0)" ::"i"(n) : "memory")
#define LD_HEAD "s_nop 0\n\t"
__device__ __forceinline__ unsigned opq(unsigned v) { asm("" : "+v"(v)); return v; }
__device__ __forceinline__ void pl1(v4u srd, unsigned vo, v4u& a) {
    asm volatile("buffer_load_dwordx4 %0, %1, %2, 0 offen sc1\n\ts_waitcnt vmcnt(0)" : "=&v"(a) : "v"(vo), "s"(srd) : "memory");
}
__device__ __forceinline__ void pl4(v4u srd, unsigned vo, unsigned s1, unsigned s2, unsigned s3, v4u& a, v4u& b, v4u& c, v4u& d) {
    asm volatile("buffer_load_dwordx4 %0, %4, %5, 0 offen sc1\n\tbuffer_load_dwordx4 %1, %4, %5, %6 offen sc1\n\t"
                 "buffer_load_dwordx4 %2, %4, %5, %7 offen sc1\n\tbuffer_load_dwordx4 %3, %4, %5, %8 offen sc1\n\ts_waitcnt vmcnt(0)"
                 : "=&v"(a), "=&v"(b), "=&v"(c), "=&v"(d) : "v"(vo), "s"(srd), "s"(s1), "s"(s2), "s"(s3) : "memory");
}
__device__ __forceinline__ bool any_lane(bool c) { return __builtin_amdgcn_ballot_w64(c) != 0; }
__device__ __forceinline__ unsigned tag_bad(v4u v, unsigned tlo, unsigned thi) { return (v.y ^ tlo) | (v.w ^ thi); }
#define ENDPGM() __builtin_amdgcn_endpgm()
#define SCHED() __builtin_amdgcn_sched_barrier(0)
#define BARRIER() asm volatile("s_waitcnt lgkmcnt(0)\n\ts_barrier" ::: "memory")
#define CLOB() asm volatile("" ::: "memory")
typedef __amdgpu_buffer_rsrc_t rsrc_t;
__device__ __forceinline__ v4u ld16(rsrc_t r, int off) { return __builtin_amdgcn_raw_buffer_load_b128(r, off, 0, 16); }
__device__ __forceinline__ v2u ld8(rsrc_t r, int off) { return __builtin_amdgcn_raw_buffer_load_b64(r, off, 0, 16); }
__device__ __forceinline__ void st16(rsrc_t r, int off, v4u v) { __builtin_amdgcn_raw_buffer_store_b128(v, r, off, 0, 16); }
__device__ __forceinline__ void st8(rsrc_t r, int off, v2u v) { __builtin_amdgcn_raw_buffer_store_b64(v, r, off, 0, 16); }
__device__ __forceinline__ float f_of(unsigned u) { return __builtin_bit_cast(float, u); }
__device__ __forceinline__ unsigned u_of(float f) { return __builtin_bit_cast(unsigned, f); }
__device__ __forceinline__ float rbf(float x) { return (float)(__bf16)x; }
__device__ __forceinline__ unsigned pk2(float a, float b) { return __builtin_bit_cast(unsigned, __builtin_convertvector(v2f{a, b}, v2bf)); }
__device__ __forceinline__ float lo16(unsigned u) { return f_of(u << 16); }
__device__ __forceinline__ float hi16(unsigned u) { return f_of(u & 0xffff0000u); }
template <int CTRL> __device__ __forceinline__ float dpp(float x) {
    return __builtin_bit_cast(float, __builtin_amdgcn_mov_dpp(__builtin_bit_cast(int, x), CTRL, 0xf, 0xf, true));
}
constexpr int X1 = 0xB1, X2 = 0x4E, ROR4 = 0x124, ROR8 = 0x128;
__device__ __forceinline__ float add16(float a, float b) { auto s = __builtin_amdgcn_permlane16_swap(u_of(a), u_of(b), false, false); return f_of(s[0]) + f_of(s[1]); }
__device__ __forceinline__ float add32(float a, float b) { auto s = __builtin_amdgcn_permlane32_swap(u_of(a), u_of(b), false, false); return f_of(s[0]) + f_of(s[1]); }
__device__ __forceinline__ float max16(float a) { auto s = __builtin_amdgcn_permlane16_swap(u_of(a), u_of(a), false, false); return fmaxf(f_of(s[0]), f_of(s[1])); }
__device__ __forceinline__ float max32(float a) { auto s = __builtin_amdgcn_permlane32_swap(u_of(a), u_of(a), false, false); return fmaxf(f_of(s[0]), f_of(s[1])); }
__device__ __forceinline__ float wave_sum(float v) {
    v += dpp<X1>(v); v += dpp<X2>(v); v += dpp<ROR4>(v); v += dpp<ROR8>(v); v = add16(v, v); return add32(v, v);
}
__device__ __forceinline__ float ex2(float x) { return __builtin_amdgcn_exp2f(x); }
__device__ __forceinline__ float fdiv(float n, float d) {
    const float r = __builtin_amdgcn_rcpf(d), q = n * r;
    return fmaf(fmaf(-q, d, n), r, q);
}
__device__ __forceinline__ float sigm(float x) { return __builtin_amdgcn_rcpf(1.f + ex2(-x * LOG2E)); }
__device__ __forceinline__ v8bf tr_frag(unsigned a0, unsigned a1) {
    const v4s t0 = __builtin_amdgcn_ds_read_tr16_b64_v4i16((LAS v4s*)a0), t1 = __builtin_amdgcn_ds_read_tr16_b64_v4i16((LAS v4s*)a1);
    return __builtin_bit_cast(v8bf, __builtin_shufflevector(t0, t1, 0, 1, 2, 3, 4, 5, 6, 7));
}
__device__ __forceinline__ v8bf lds_frag(unsigned a) { return *(LAS v8bf*)a; }
__device__ __forceinline__ v4f mfma(v8bf a, v8bf b, v4f c) { return __builtin_amdgcn_mfma_f32_16x16x32_bf16(a, b, c, 0, 0, 0); }
__device__ __forceinline__ bool tag_ok(v4u v, unsigned tlo, unsigned thi) { return v.y == tlo && v.w == thi; }
__device__ __forceinline__ unsigned umax4(v4u v) { return max(max(v.x, v.y), max(v.z, v.w)); }
__device__ __forceinline__ u64 rt() { return __builtin_amdgcn_s_memrealtime(); }

#define STAMP(k) do {} while (0)
#define STAMP_OUT() do {} while (0)

__device__ __forceinline__ unsigned tile_rd_addr(unsigned base, int w, int lane) {
    const int G = lane >> 4, q = (lane & 15) >> 2, p = lane & 3;
    return base + (8 * G + q) * 128 + (((2 * w + (p >> 1)) ^ (2 * ((q >> 1) | ((G & 1) << 1)))) << 4) + (p & 1) * 8;
}
__device__ __forceinline__ unsigned tile_dma_voff(int pitch, int w, int lane) {
    const int r8 = lane >> 3, p = lane & 7;
    return r8 * pitch + ((p ^ (2 * (((r8 >> 1) & 1) | ((w & 1) << 1)))) << 4);
}
__device__ __forceinline__ void silu_to_lds(unsigned cw, int tid) {
    const float a = lo16(cw), b = hi16(cw);
    *(LAS unsigned*)(L_SC + (tid & 127) * 4) = pk2(a * sigm(a), b * sigm(b));
}
__device__ __forceinline__ void mod_tile(unsigned base, rsrc_t wsr, int cb, int kh, int w, int lane, unsigned tlo, unsigned thi) {
    const unsigned ra = opq(tile_rd_addr(base, w, lane)), rb = opq(L_SC + 16 * (lane >> 4));
    v8bf wf[8], af[8];
#pragma unroll
    for (int t = 0; t < 8; ++t) { wf[t] = tr_frag(ra + t * 4096, ra + t * 4096 + 512); af[t] = lds_frag(rb + 64 * t); }
    SCHED();
    v4f d = {0.f, 0.f, 0.f, 0.f};
#pragma unroll
    for (int t = 0; t < 8; ++t) d = mfma(wf[t], af[t], d);
    const int e = lane & 15;
    if (e < 2) st16(wsr, WS_MOD + kh * MOD_HALF + (32 * cb + 8 * w + 2 * (lane >> 4) + e) * 16, v4u{u_of(e ? d.z : d.x), tlo, u_of(e ? d.w : d.y), thi});
}

#define TAG_FROM(ev) const u64 T_ = (((u64)(unsigned)__builtin_amdgcn_readfirstlane((ev).y) << 32) | (unsigned)__builtin_amdgcn_readfirstlane((ev).x)) + 1
__device__ __forceinline__ v2u tag_from_lds() {
    const v2u t = *(LAS v2u*)L_TAG;
    return v2u{(unsigned)__builtin_amdgcn_readfirstlane(t.x), (unsigned)__builtin_amdgcn_readfirstlane(t.y)};
}

__global__ void __launch_bounds__(512, 2) __attribute__((amdgpu_waves_per_eu(2, 2))) fused_block(Params p_) {
    typedef unsigned v16u __attribute__((ext_vector_type(16))); typedef unsigned v8u __attribute__((ext_vector_type(8)));
    u64 t_start, t_k; v16u ka; v8u kb;
    asm volatile("s_memrealtime %0\n\ts_waitcnt lgkmcnt(0)" : "=s"(t_start) :: "memory");
    asm volatile("s_load_dwordx16 %0, %2, 0\n\ts_load_dwordx8 %1, %2, 64\n\ts_waitcnt lgkmcnt(0)" : "=&s"(ka), "=&s"(kb) : "s"(__builtin_amdgcn_kernarg_segment_ptr()) : "memory");
    asm volatile("s_memrealtime %0\n\ts_waitcnt lgkmcnt(0)" : "=s"(t_k) :: "memory");
#define PTR(v, i) ((u16*)(((u64)v[2 * (i) + 1] << 32) | v[2 * (i)]))
    Params p;
    p.x = PTR(ka, 0); p.kc = PTR(ka, 1); p.vc = PTR(ka, 2); p.wq = PTR(ka, 3); p.wo = PTR(ka, 4); p.wg = PTR(ka, 5); p.wu = PTR(ka, 6); p.wd = PTR(ka, 7);
    p.c = PTR(kb, 0); p.wm = PTR(kb, 1); p.out = PTR(kb, 2); p.ws = (unsigned char*)PTR(kb, 3);
    const int b = blockIdx.x, xcd = b & 7, ib = b >> 3, tid = threadIdx.x, lane = tid & 63;
    const int w = __builtin_amdgcn_readfirstlane(tid >> 6);
    const int G = lane >> 4, c16 = lane & 15;
    STAMP(0);
    const rsrc_t wsr = __builtin_amdgcn_make_buffer_rsrc(p.ws, 0, WS_END, 0x20000);
    const v4u wsd = mk_srd(p.ws, WS_END);
    if (ib < 16) {
        const int g = xcd, s = ib;
        if (w < 4) {
            const size_t hb = ((size_t)g * SK + (SK - WIN)) * 128;
            const unsigned so0 = (KV_INTERLEAVE ? s * 4096 : s * 65536) + w * 1024, gs = KV_INTERLEAVE ? 65536 : 4096;
            dma_seq<16, 4096>(mk_srd(p.kc + hb, 1 << 20), G * 256 + ((c16 ^ (4 * w + G)) << 4), so0, gs, L_T0 + w * 1024);
            dma_seq<16, 4096>(mk_srd(p.vc + hb, 1 << 20), G * 256 + ((c16 ^ (2 * G | ((w & 2) << 2))) << 4), so0, gs, L_T1 + w * 1024);
            STAMP(1);
            WAIT_VM(16);
        } else if (w == 7) {
            const v4u s4 = {SENT, SENT, SENT, SENT};
            st16(wsr, WS_H3 + ((4 * g + (lane >> 5)) * 16 + s) * 512 + (lane & 31) * 16, s4);
            st16(wsr, WS_H3 + ((4 * g + 2 + (lane >> 5)) * 16 + s) * 512 + (lane & 31) * 16, s4);
            if (lane < 4) st8(wsr, WS_ML + ((4 * g + lane) * 16 + s) * 8, v2u{SENT, SENT});
            if (lane < 16) st16(wsr, WS_H4 + (2 * g + (s >> 3)) * 2048 + (s & 7) * 256 + lane * 16, s4);
            v2u ev;
            asm volatile("buffer_load_dwordx2 %0, off, %1, 0 sc1\n\ts_waitcnt vmcnt(0)" : "=&v"(ev) : "s"(wsd) : "memory");
            TAG_FROM(ev);
            STAMP(1);
            if (lane == 0) { st16(wsr, WS_FLGA + (16 * g + s) * 16, v4u{0u, (unsigned)T_, 0u, (unsigned)(T_ >> 32)}); *(LAS v2u*)L_TAG = v2u{(unsigned)T_, (unsigned)(T_ >> 32)}; }
        }
        STAMP(2);
        BARRIER();
        v8bf kf[2][4];
#pragma unroll
        for (int t = 0; t < 4; ++t) {
            const unsigned a = L_T0 + (32 * w + c16) * 256 + (((4 * t + G) ^ c16) << 4);
            kf[0][t] = lds_frag(a); kf[1][t] = lds_frag(a + 4096);
        }
        const int ks = 2 * g + (s >> 3), j = s & 7;
        const unsigned qra = opq(L_Q + (lane & 3) * 320 + 16 * G), pra = opq(L_P + (lane & 3) * 576 + 16 * G), sra = opq(L_ST + (lane & 3) * 64);
        const unsigned vra = opq(L_T1 + (8 * G + (c16 >> 2)) * 256 + (((2 * w + ((lane & 3) >> 1)) ^ (2 * (c16 >> 2) | ((G & 1) << 3))) << 4) + (lane & 1) * 8);
        const unsigned pwa = opq(L_P + (lane & 3) * 576 + (32 * w + ((lane & 4) << 2) + 4 * G + ((lane & 8) >> 2)) * 2);
        const unsigned h3st = opq(WS_H3 + ((4 * g + (lane & 3)) * 16 + s) * 512 + (16 * w + 4 * G) * 4);
        const unsigned wovo = opq(tile_dma_voff(1024, w, lane));
        if (w < 4) {
            const v2u tg = tag_from_lds();
            const unsigned qoff = lane < 32 ? WS_Q + (32 * (4 * g + w) + lane) * 16 : WS_FLGA + (16 * g + c16) * 16;
            const unsigned qwa = opq(L_Q + w * 320 + (lane & 31) * 8);
            WAIT_VM(Q_POLL_VM);
            STAMP(11);
            v4u qv;
#pragma unroll 1
            for (int it = 0; it < SPIN_CAP; ++it) { pl1(wsd, qoff, qv); if (!any_lane(tag_bad(qv, tg.x, tg.y) != 0)) break; }
            if (lane < 32) *(LAS v2u*)qwa = v2u{qv.x, qv.z};
        }
        STAMP(3);
        BARRIER();
        STAMP(4);
        v8bf qf[4], vf[8];
#pragma unroll
        for (int t = 0; t < 4; ++t) qf[t] = lds_frag(qra + 64 * t);
        if (w < 4) dma_seq<8, 4096>(mk_srd(p.wo + (size_t)ks * 256 * 512 + 64 * j, 256 * 1024), wovo, w * 8192, 32768, L_T0 + w * 1024);
#pragma unroll
        for (int t = 0; t < 8; ++t) vf[t] = tr_frag(vra + t * 8192, vra + t * 8192 + 1024);
        SCHED();
        v4f d0 = {0.f, 0.f, 0.f, 0.f}, d1 = d0;
#pragma unroll
        for (int t = 0; t < 4; ++t) { d0 = mfma(kf[0][t], qf[t], d0); d1 = mfma(kf[1][t], qf[t], d1); }
        {
            const bool tau = lane & 4, pr = lane & 8;
            const float a0 = pr ? d0.z : d0.x, a1 = pr ? d0.w : d0.y, b0 = pr ? d1.z : d1.x, b1 = pr ? d1.w : d1.y;
            const float s0 = rbf(rbf(tau ? b0 : a0) * 0.08838834764831845f), s1 = rbf(rbf(tau ? b1 : a1) * 0.08838834764831845f);
            float m = fmaxf(s0, s1);
            m = fmaxf(m, dpp<ROR4>(m)); m = fmaxf(m, dpp<ROR8>(m)); m = max16(m); m = max32(m);
            const float p0 = ex2((s0 - m) * LOG2E), p1 = ex2((s1 - m) * LOG2E);
            *(LAS unsigned*)pwa = pk2(p0, p1);
            float l = p0 + p1;
            l += dpp<ROR4>(l); l += dpp<ROR8>(l); l = add16(l, l); l = add32(l, l);
            if (lane < 4) { *(LAS float*)(sra + w * 4) = m; *(LAS float*)(sra + 32 + w * 4) = l; }
        }
        STAMP(5);
        BARRIER();
        {
            v8bf pf[8];
            const v4f ma = *(const LAS v4f*)sra, mb = *(const LAS v4f*)(sra + 16);
#pragma unroll
            for (int t = 0; t < 8; ++t) pf[t] = lds_frag(pra + 64 * t);
            const v4f la = *(const LAS v4f*)(sra + 32), lb = *(const LAS v4f*)(sra + 48);
            SCHED();
            v4f dv[8];
#pragma unroll
            for (int t = 0; t < 8; ++t) dv[t] = mfma(vf[t], pf[t], v4f{0.f, 0.f, 0.f, 0.f});
            const float M = fmaxf(fmaxf(fmaxf(ma.x, ma.y), fmaxf(ma.z, ma.w)), fmaxf(fmaxf(mb.x, mb.y), fmaxf(mb.z, mb.w)));
            float f[8];
#pragma unroll
            for (int t = 0; t < 4; ++t) { f[t] = ex2((ma[t] - M) * LOG2E); f[4 + t] = ex2((mb[t] - M) * LOG2E); }
            v4f o = dv[0] * f[0];
#pragma unroll
            for (int t = 1; t < 8; ++t) o += dv[t] * f[t];
            if (c16 < 4) st16(wsr, h3st, __builtin_bit_cast(v4u, o));
            if (w == 4) {
                float L = 0.f;
#pragma unroll
                for (int t = 0; t < 4; ++t) L += f[t] * la[t] + f[4 + t] * lb[t];
                if (lane < 4) st8(wsr, WS_ML + ((4 * g + lane) * 16 + s) * 8, v2u{u_of(M), u_of(L)});
            }
        }
        STAMP(6);
        const int hd = 2 * ks + (w >> 2), s8 = lane >> 3;
        const unsigned o3 = WS_H3 + (hd * 16 + s8) * 512 + (w & 3) * 128 + (lane & 7) * 16, oml = WS_ML + (hd * 16 + s8) * 8;
        const unsigned owa = opq(L_O + ((w >> 2) * 128 + (w & 3) * 32 + 4 * (lane & 7) + G) * 2), ora = opq(L_O + 16 * G);
        const unsigned h4st = opq(WS_H4 + ks * 2048 + (64 * j + 16 * w + 4 * G) * 4);
        if (w < 4) WAIT_VM(1);
        BARRIER();
        STAMP(7);
        v8bf wf[8];
        {
            const unsigned ra = opq(tile_rd_addr(L_T0, w & 3, lane));
#pragma unroll
            for (int t = 0; t < 8; ++t) wf[t] = tr_frag(ra + t * 4096, ra + t * 4096 + 512);
        }
        v4u a0, a1; v2u m0, m1;
#pragma unroll 1
        for (int it = 0; it < SPIN_CAP; ++it) {
            asm volatile("buffer_load_dwordx2 %2, %5, %6, 0 offen sc1\n\tbuffer_load_dwordx2 %3, %5, %6, 0 offen offset:64 sc1\n\t"
                         "buffer_load_dwordx4 %0, %4, %6, 0 offen sc1\n\tbuffer_load_dwordx4 %1, %4, %6, %7 offen sc1\n\ts_waitcnt vmcnt(0)"
                         : "=&v"(a0), "=&v"(a1), "=&v"(m0), "=&v"(m1) : "v"(o3), "v"(oml), "s"(wsd), "s"(8 * 512) : "memory");
            const unsigned mx = max(max(umax4(a0), umax4(a1)), max(max(m0.x, m0.y), max(m1.x, m1.y)));
            if (!any_lane(mx == SENT)) break;
        }
        STAMP(8);
        {
            float M = fmaxf(f_of(m0.x), f_of(m1.x));
            M = fmaxf(M, dpp<ROR8>(M)); M = max16(M); M = max32(M);
            const float w0 = ex2((f_of(m0.x) - M) * LOG2E), w1 = ex2((f_of(m1.x) - M) * LOG2E);
            float L = w0 * f_of(m0.y) + w1 * f_of(m1.y);
            const v4f v = __builtin_bit_cast(v4f, a0) * w0 + __builtin_bit_cast(v4f, a1) * w1;
            L += dpp<ROR8>(L); L = add16(L, L); L = add32(L, L);
            float z = add16(add32(v.x, v.z), add32(v.y, v.w));
            z += dpp<ROR8>(z);
            *(LAS u16*)owa = (u16)pk2(fdiv(z, L), 0.f);
        }
        BARRIER();
        STAMP(9);
        if (w < 4) {
            v8bf of[8];
#pragma unroll
            for (int t = 0; t < 8; ++t) of[t] = lds_frag(ora + 64 * t);
            SCHED();
            v4f d = {0.f, 0.f, 0.f, 0.f};
#pragma unroll
            for (int t = 0; t < 8; ++t) d = mfma(wf[t], of[t], d);
            if (c16 == 0) st16(wsr, h4st, __builtin_bit_cast(v4u, d));
        }
        if (w == 0) { const v2u tq_ = tag_from_lds(); const u64 tn_ = rt(); if (lane == 0) { st16(wsr, WS_EXP + b * 16, v4u{(unsigned)t_start, tq_.x, 0u, tq_.y}); st16(wsr, WS_EXP + 4096 + b * 16, v4u{(unsigned)tn_, tq_.x, 0u, tq_.y}); } }
        ENDPGM();
    }
    if (ib < 24) {
        const int g = xcd, j = ib - 16, mt = xcd * 8 + j, cb = 16 + (mt >> 1), kh = mt & 1;
        if (w < 4) {
            dma_seq<16, 4096>(mk_srd(p.wq + 512 * g + 64 * j, 512 * 8192), tile_dma_voff(8192, w, lane), w * 65536, 4 * 65536, L_T0 + w * 1024);
            STAMP(1);
            WAIT_VM(0);
            STAMP(2);
            BARRIER();
            v8bf wf[16];
            const unsigned ra = opq(tile_rd_addr(L_T0, w, lane)), rb = opq(L_ACT + 16 * G);
#pragma unroll
            for (int t = 0; t < 16; ++t) wf[t] = tr_frag(ra + t * 4096, ra + t * 4096 + 512);
            const v2u tg = tag_from_lds();
            const unsigned qst = opq(WS_Q + (128 * g + 16 * j + 4 * w + G) * 16), mvo = opq(tile_dma_voff(6144, w, lane));
            BARRIER();
            STAMP(4);
            v8bf hf[16];
#pragma unroll
            for (int t = 0; t < 16; ++t) hf[t] = lds_frag(rb + 64 * t);
            SCHED();
            v4f d = {0.f, 0.f, 0.f, 0.f};
#pragma unroll
            for (int t = 0; t < 16; ++t) d = mfma(wf[t], hf[t], d);
            if (c16 == 0) st16(wsr, qst, v4u{pk2(d.x, d.y), tg.x, pk2(d.z, d.w), tg.y});
            STAMP(5);
            dma_seq<8, 4096>(mk_srd(p.wm + (size_t)kh * 256 * 3072 + 64 * cb, 256 * 6144), mvo, w * 8 * 6144, 32 * 6144, L_T1 + w * 1024);
            WAIT_VM(0);
            BARRIER();
            STAMP(6);
            mod_tile(L_T1, wsr, cb, kh, w, lane, tg.x, tg.y);
            if (w == 0) { const v2u tq_ = tag_from_lds(); const u64 tn_ = rt(); if (lane == 0) { st16(wsr, WS_EXP + b * 16, v4u{(unsigned)t_start, tq_.x, 0u, tq_.y}); st16(wsr, WS_EXP + 4096 + b * 16, v4u{(unsigned)tn_, tq_.x, 0u, tq_.y}); } }
            ENDPGM();
        }
        const int hp = 64 * (w & 3) + lane;
        v2u ev; v4u x8; unsigned x2w, cw;
        asm volatile("buffer_load_dwordx2 %0, off, %4, 0 sc1\n\tglobal_load_dwordx4 %1, %5, %8\n\tglobal_load_dword %2, %6, %8\n\tglobal_load_dword %3, %7, %9\n\ts_waitcnt vmcnt(0)"
                     : "=&v"(ev), "=&v"(x8), "=&v"(x2w), "=&v"(cw)
                     : "s"(wsd), "v"(lane * 16), "v"(hp * 4), "v"((128 * kh + (tid & 127)) * 4), "s"(p.x), "s"(p.c) : "memory");
        TAG_FROM(ev);
        const unsigned tlo = (unsigned)T_, thi = (unsigned)(T_ >> 32);
        *(LAS v2u*)L_TAG = v2u{tlo, thi};
        STAMP(1);
        silu_to_lds(cw, tid);
        float xs[8];
#pragma unroll
        for (int k = 0; k < 4; ++k) { xs[2 * k] = lo16(x8[k]); xs[2 * k + 1] = hi16(x8[k]); }
        float sum = 0.f;
#pragma unroll
        for (int k = 0; k < 8; ++k) sum += xs[k];
        const float mean = wave_sum(sum) * (1.f / 512.f);
        float sq = 0.f;
#pragma unroll
        for (int k = 0; k < 8; ++k) sq = fmaf(xs[k] - mean, xs[k] - mean, sq);
        const float var = rbf(wave_sum(sq) * (1.f / 512.f)), mr = rbf(mean), den = rbf(__builtin_amdgcn_sqrtf(rbf(var + 1e-5f)));
        const float ln0 = rbf(fdiv(rbf(lo16(x2w) - mr), den)), ln1 = rbf(fdiv(rbf(hi16(x2w) - mr), den));
        const unsigned hwa = opq(L_ACT + hp * 4);
        BARRIER();
        STAMP(2);
        v4u sa0, sa1, ca0, ca1;
#pragma unroll 1
        for (int it = 0; it < SPIN_CAP; ++it) {
            pl4(wsd, WS_MOD + hp * 16, MOD_HALF, 4096, 4096 + MOD_HALF, sa0, sa1, ca0, ca1);
            if (!any_lane((tag_bad(sa0, tlo, thi) | tag_bad(sa1, tlo, thi) | tag_bad(ca0, tlo, thi) | tag_bad(ca1, tlo, thi)) != 0)) break;
        }
        STAMP(3);
        const float h0 = rbf(ln0 * rbf(1.f + rbf(f_of(ca0.x) + f_of(ca1.x)))) + rbf(f_of(sa0.x) + f_of(sa1.x));
        const float h1 = rbf(ln1 * rbf(1.f + rbf(f_of(ca0.z) + f_of(ca1.z)))) + rbf(f_of(sa0.z) + f_of(sa1.z));
        *(LAS unsigned*)hwa = pk2(h0, h1);
        BARRIER();
        STAMP(4);
        STAMP_OUT();
        BARRIER();
        ENDPGM();
    }
    {
        const int u = ib - 24;
        const int f = xcd < 4 ? 8 * xcd + u : (u < 6 ? 32 + 6 * (xcd - 4) + u : -1);
        if (f < 0) ENDPGM();
        const u64 t_in = F_DELAY ? rt() : 0;
        const int mt = xcd * 4 + u, cb = mt >> 1, kh = mt & 1;
        if (w < 4) {
            if (u < 4) {
                dma_seq<8, 4096>(mk_srd(p.wm + (size_t)kh * 256 * 3072 + 64 * cb, 256 * 6144), tile_dma_voff(6144, w, lane), w * 8 * 6144, 32 * 6144, L_T2 + w * 1024);
                STAMP(1);
                WAIT_VM(0);
            }
        } else {
            v2u ev; unsigned cw;
            asm volatile("buffer_load_dwordx2 %0, off, %2, 0 sc1\n\tglobal_load_dword %1, %3, %4\n\ts_waitcnt vmcnt(0)"
                         : "=&v"(ev), "=&v"(cw) : "s"(wsd), "v"((128 * kh + (tid & 127)) * 4), "s"(p.c) : "memory");
            TAG_FROM(ev);
            *(LAS v2u*)L_TAG = v2u{(unsigned)T_, (unsigned)(T_ >> 32)};
            silu_to_lds(cw, tid);
            STAMP(1);
        }
        BARRIER();
        STAMP(2);
        const v2u tg = tag_from_lds();
        const unsigned tlo = tg.x, thi = tg.y;
        if (w < 4) { if (u < 4) mod_tile(L_T2, wsr, cb, kh, w, lane, tlo, thi); }
        else if (w == 7) {
            const v4u s4 = {SENT, SENT, SENT, SENT};
            st16(wsr, WS_H6 + f * 2048 + lane * 16, s4);
            st16(wsr, WS_H6 + f * 2048 + 1024 + lane * 16, s4);
        }
        BARRIER();
        STAMP(3);
        if (F_DELAY) { while (rt() - t_in < F_DELAY) __builtin_amdgcn_s_sleep(2); }
        const int col = 64 * w + 4 * c16 + G, colr = 32 * f + 4 * (lane & 7) + G;
        if (w < 4) {
            const unsigned vo = (lane >> 2) * 3584, pz = (lane & 3) ^ (lane >> 5);
            dma_seq<8, 4096>(mk_srd(p.wg + 32 * f, 512 * 3584), vo + (pz << 4), w * 16 * 3584, 64 * 3584, L_T0 + w * 1024);
            dma_seq<8, 4096>(mk_srd(p.wu + 32 * f, 512 * 3584), vo + ((pz ^ 2) << 4), w * 16 * 3584, 64 * 3584, L_T0 + 32768 + w * 1024);
            dma_seq<8, 4096>(mk_srd(p.wd + (size_t)32 * f * 512, 32 * 1024), (lane ^ (2 * w)) << 4, w * 1024, 4096, L_T2 + w * 1024);
        } else if (w == 7) {
            WAIT_VM(0);
            if (lane == 0) st16(wsr, WS_FLGF + f * 16, v4u{0u, tlo, 0u, thi});
        }
        BARRIER();
        STAMP(4);
        const bool red = f < 16 && w == 7;
        float ga, sb, cb1, gb, xi;
        {
            const unsigned om = WS_MOD + (512 + (col >> 1)) * 16, of = WS_FLGA + (16 * w + c16) * 16;
            const unsigned og = WS_MOD + (1280 + ((colr & 511) >> 1)) * 16, off = WS_FLGF + (lane < 56 ? lane : 0) * 16;
            v4u g0, g1, s0, s1, c0, c1, fa, b0, b1, fb; unsigned xc;
#pragma unroll 1
            for (int it = 0; it < SPIN_CAP; ++it) {
                asm volatile("buffer_load_dwordx4 %0, %11, %15, 0 offen sc1\n\tbuffer_load_dwordx4 %1, %11, %15, %16 offen sc1\n\t"
                             "buffer_load_dwordx4 %2, %11, %15, %17 offen sc1\n\tbuffer_load_dwordx4 %3, %11, %15, %18 offen sc1\n\t"
                             "buffer_load_dwordx4 %4, %11, %15, %19 offen sc1\n\tbuffer_load_dwordx4 %5, %11, %15, %20 offen sc1\n\t"
                             "buffer_load_dwordx4 %6, %12, %15, 0 offen sc1\n\tbuffer_load_dwordx4 %7, %13, %15, 0 offen sc1\n\t"
                             "buffer_load_dwordx4 %8, %13, %15, %16 offen sc1\n\tbuffer_load_dwordx4 %9, %14, %15, 0 offen sc1\n\t"
                             "global_load_ushort %10, %21, %22\n\ts_waitcnt vmcnt(0)"
                             : "=&v"(g0), "=&v"(g1), "=&v"(s0), "=&v"(s1), "=&v"(c0), "=&v"(c1), "=&v"(fa), "=&v"(b0), "=&v"(b1), "=&v"(fb), "=&v"(xc)
                             : "v"(om), "v"(of), "v"(og), "v"(off), "s"(wsd), "s"(MOD_HALF), "s"(4096), "s"(4096 + MOD_HALF), "s"(8192), "s"(8192 + MOD_HALF),
                               "v"(col * 2), "s"(p.x) : "memory");
                const unsigned bad = tag_bad(g0, tlo, thi) | tag_bad(g1, tlo, thi) | tag_bad(s0, tlo, thi) | tag_bad(s1, tlo, thi) | tag_bad(c0, tlo, thi) |
                                     tag_bad(c1, tlo, thi) | tag_bad(fa, tlo, thi) | tag_bad(b0, tlo, thi) | tag_bad(b1, tlo, thi) | tag_bad(fb, tlo, thi);
                if (!any_lane(bad != 0)) break;
                __builtin_amdgcn_s_sleep(2);
            }
            const bool od = G & 1;
            ga = rbf(od ? f_of(g0.z) + f_of(g1.z) : f_of(g0.x) + f_of(g1.x));
            sb = rbf(od ? f_of(s0.z) + f_of(s1.z) : f_of(s0.x) + f_of(s1.x));
            cb1 = rbf(1.f + rbf(od ? f_of(c0.z) + f_of(c1.z) : f_of(c0.x) + f_of(c1.x)));
            gb = rbf(od ? f_of(b0.z) + f_of(b1.z) : f_of(b0.x) + f_of(b1.x));
            xi = lo16(xc);
        }
        STAMP(5);
        BARRIER();
        STAMP(6);
        v8bf gf[16], df[4];
        {
            const int q = c16 >> 2, pp = lane & 3;
            const unsigned ra = opq(L_T0 + (pp >> 1) * 32768 + (8 * G + q) * 64 + (((w & 3) ^ ((G & 1) | (pp & 2))) << 4) + (pp & 1) * 8);
#pragma unroll
            for (int t = 0; t < 16; ++t) gf[t] = tr_frag(ra + t * 2048, ra + t * 2048 + 256);
#pragma unroll
            for (int t = 0; t < 4; ++t) {
                const unsigned rd = opq(L_T2 + (8 * G + q) * 1024 + ((8 * w + ((2 * t) ^ (2 * q)) + (pp >> 1)) << 4) + (pp & 1) * 8);
                df[t] = tr_frag(rd, rd + 4096);
            }
        }
        const unsigned x2wa = opq(L_X2 + col * 4), hwa = opq(L_ACT + col * 2), hra = opq(L_ACT + 16 * G), awa = opq(L_A2 + (8 * w + 4 * (G & 1) + (lane & 3)) * 2);
        const unsigned ara = opq(L_A2 + 16 * G), h6st = opq(WS_H6 + f * 2048 + (64 * w + 4 * G) * 4), rwa = opq(L_RED + w * 4), rra = opq(L_RED);
        v4u a0, a1, a2, a3;
#pragma unroll 1
        for (int it = 0; it < SPIN_CAP; ++it) {
            pl4(wsd, WS_H4 + G * 2048 + (64 * w + 4 * c16) * 4, 4 * 2048, 8 * 2048, 12 * 2048, a0, a1, a2, a3);
            const unsigned mx = max(max(umax4(a0), umax4(a1)), max(umax4(a2), umax4(a3)));
            if (!any_lane(mx == SENT)) break;
        }
        STAMP(7);
        const v4f sv = (__builtin_bit_cast(v4f, a0) + __builtin_bit_cast(v4f, a1)) + (__builtin_bit_cast(v4f, a2) + __builtin_bit_cast(v4f, a3));
        const float ao = rbf(add16(add32(sv.x, sv.z), add32(sv.y, sv.w)));
        const float x2 = rbf(xi + rbf(ga * ao));
        {
            const float s1 = wave_sum(x2), s2 = wave_sum(x2 * x2);
            *(LAS float*)rwa = s1; *(LAS float*)(rwa + 32) = s2;
            *(LAS float*)x2wa = x2;
        }
        BARRIER();
        STAMP(8);
        {
            const LAS v4f* rp = (const LAS v4f*)rra;
            const v4f r0 = rp[0] + rp[1], r1 = rp[2] + rp[3];
            const float mean = ((r0.x + r0.y) + (r0.z + r0.w)) * (1.f / 512.f), ex = ((r1.x + r1.y) + (r1.z + r1.w)) * (1.f / 512.f);
            const float var = rbf(fmaxf(fmaf(-mean, mean, ex), 0.f)), den = rbf(__builtin_amdgcn_sqrtf(rbf(var + 1e-5f)));
            const float ln = rbf(fdiv(rbf(x2 - rbf(mean)), den));
            *(LAS u16*)hwa = (u16)pk2(rbf(ln * cb1) + sb, 0.f);
        }
        BARRIER();
        STAMP(9);
        if (w < 4) {
            v8bf hf[16];
#pragma unroll
            for (int t = 0; t < 16; ++t) hf[t] = lds_frag(hra + 64 * t);
            SCHED();
            v4f d = {0.f, 0.f, 0.f, 0.f};
#pragma unroll
            for (int t = 0; t < 16; ++t) d = mfma(gf[t], hf[t], d);
            const float v = (lane & 2) ? ((lane & 1) ? d.w : d.z) : ((lane & 1) ? d.y : d.x);
            const auto sw = __builtin_amdgcn_permlane32_swap(u_of(v), u_of(v), false, false);
            const float gt = rbf(f_of(sw[0])), up = rbf(f_of(sw[1]));
            const float ge = rbf(gt * sigm(1.5957691216057308f * fmaf(0.044715f * gt * gt, gt, gt)));
            *(LAS u16*)awa = (u16)pk2(ge * up, 0.f);
        }
        BARRIER();
        STAMP(10);
        {
            const v8bf af = lds_frag(ara);
            v4f d[4];
#pragma unroll
            for (int t = 0; t < 4; ++t) d[t] = mfma(df[t], af, v4f{0.f, 0.f, 0.f, 0.f});
            if (c16 == 0) {
#pragma unroll
                for (int t = 0; t < 4; ++t) st16(wsr, h6st + 64 * t, __builtin_bit_cast(v4u, d[t]));
            }
        }
        if (w == 0) { const v2u tq_ = tag_from_lds(); const u64 tn_ = rt(); if (lane == 0) { st16(wsr, WS_EXP + b * 16, v4u{(unsigned)t_start, tq_.x, 0u, tq_.y}); st16(wsr, WS_EXP + 4096 + b * 16, v4u{(unsigned)tn_, tq_.x, 0u, tq_.y}); } }
        if (red) {
            const float xr = *(LAS float*)(L_X2 + colr * 4);
            const unsigned o6 = WS_H6 + (lane >> 3) * 2048 + (32 * f + 4 * (lane & 7)) * 4;
            v4u r0, r1, r2, r3, r4, r5, r6;
#pragma unroll 1
            for (int it = 0; it < SPIN_CAP; ++it) {
                asm volatile("buffer_load_dwordx4 %0, %7, %8, 0 offen sc1\n\tbuffer_load_dwordx4 %1, %7, %8, %9 offen sc1\n\t"
                             "buffer_load_dwordx4 %2, %7, %8, %10 offen sc1\n\tbuffer_load_dwordx4 %3, %7, %8, %11 offen sc1\n\t"
                             "buffer_load_dwordx4 %4, %7, %8, %12 offen sc1\n\tbuffer_load_dwordx4 %5, %7, %8, %13 offen sc1\n\t"
                             "buffer_load_dwordx4 %6, %7, %8, %14 offen sc1\n\ts_waitcnt vmcnt(0)"
                             : "=&v"(r0), "=&v"(r1), "=&v"(r2), "=&v"(r3), "=&v"(r4), "=&v"(r5), "=&v"(r6)
                             : "v"(o6), "s"(wsd), "s"(16384), "s"(2 * 16384), "s"(3 * 16384), "s"(4 * 16384), "s"(5 * 16384), "s"(6 * 16384) : "memory");
                const unsigned mx = max(max(max(umax4(r0), umax4(r1)), max(umax4(r2), umax4(r3))), max(max(umax4(r4), umax4(r5)), umax4(r6)));
                if (!any_lane(mx == SENT)) break;
            }
            STAMP(12);
            const v4f v = ((__builtin_bit_cast(v4f, r0) + __builtin_bit_cast(v4f, r1)) + (__builtin_bit_cast(v4f, r2) + __builtin_bit_cast(v4f, r3))) +
                          ((__builtin_bit_cast(v4f, r4) + __builtin_bit_cast(v4f, r5)) + __builtin_bit_cast(v4f, r6));
            float z = add16(add32(v.x, v.z), add32(v.y, v.w));
            z += dpp<ROR8>(z);
            if (!(lane & 8)) ((__attribute__((address_space(1))) u16*)p.out)[colr] = (u16)pk2(xr + rbf(gb * rbf(z)), 0.f);
            if (f == 0 && lane == 0) st8(wsr, WS_E, v2u{tlo, thi});
            if (f == 0) {
                unsigned X = 0;
                const unsigned t0 = (unsigned)t_start;
                if (0 == 1) X = (unsigned)rt() - t0;
                if (0 >= 2) {
                    int mn = 1 << 30, mx = -(1 << 30);
                    for (int k = 0; k < 4; ++k) {
                        const int bb = k * 64 + lane; const bool val = !((bb >> 3) >= 30 && (bb & 7) >= 4);
                        const bool sel = 0 == 2 || 0 == 3 ? true : 0 == 4 ? (bb >> 3) < 16 : 0 == 5 ? ((bb >> 3) >= 16 && (bb >> 3) < 24) : true;
                        v4u e;
                        for (int it = 0; it < SPIN_CAP; ++it) { pl1(wsd, WS_EXP + (0 >= 4 ? 4096 : 0) + bb * 16, e); if (!any_lane(val && tag_bad(e, tlo, thi) != 0)) break; }
                        if (val && sel) { const int d = (int)(e.x - t0); mn = min(mn, d); mx = max(mx, d); }
                    }
                    for (int o = 32; o; o >>= 1) { mn = min(mn, __shfl_xor(mn, o)); mx = max(mx, __shfl_xor(mx, o)); }
                    X = 0 == 3 ? -mn : mx;
                    X = __builtin_amdgcn_readfirstlane(X);
                }
                while ((unsigned)rt() - t0 < 4000u + X) __builtin_amdgcn_s_sleep(1);
            }
        }
        STAMP_OUT();
    }
}

extern "C" void kernel_launch(void* const* d_in, const int* in_sizes, int n_in, void* d_out, int out_size, void* d_ws, size_t ws_size,
                              hipStream_t stream) {
    Params p;
    p.x = (const u16*)d_in[0]; p.kc = (const u16*)d_in[1]; p.vc = (const u16*)d_in[2]; p.wq = (const u16*)d_in[3]; p.wo = (const u16*)d_in[4];
    p.wg = (const u16*)d_in[6]; p.wu = (const u16*)d_in[7]; p.wd = (const u16*)d_in[8]; p.c = (const u16*)d_in[9]; p.wm = (const u16*)d_in[10];
    p.out = (u16*)d_out; p.ws = (unsigned char*)d_ws;
    (void)hipFuncSetAttribute((const void*)fused_block, hipFuncAttributeMaxDynamicSharedMemorySize, LDS_BYTES);
    hipLaunchKernelGGL(fused_block, dim3(256), dim3(512), LDS_BYTES, stream, p);
}
```

```cpp
#include <hip/hip_runtime.h>
#include <stdint.h>

typedef unsigned short u16;
struct Params { const u16 *x, *kc, *vc, *wq, *wo, *wg, *wu, *wd, *c, *wm; u16* out; unsigned char* ws; };
constexpr int LDS_BYTES = 139776;
#define WS_OFF(p, n) (((((uintptr_t)(p) + (n) - (2u << 20)) + 16383) & ~(uintptr_t)16383) - (uintptr_t)(p))

__global__ void __launch_bounds__(512, 2) fused_block(Params p) {
    asm volatile(
        "s_mov_b64 s[0:1], %0\n"
        "s_mov_b32 s2, %1\n"
        "v_mov_b32 v0, %2\n"
        "s_load_dwordx16 s[8:23], s[0:1], 0\n"
        "s_load_dwordx8 s[24:31], s[0:1], 64\n"
        "s_getpc_b64 s[52:53]\n"
        ".Lfb_KSTART:\n"
        "v_lshlrev_b32 v5, 7, v0\n"
        "v_min_u32 v5, .Lfb_KEND-.Lfb_KSTART-4, v5\n"
        "global_load_dword v239, v5, s[52:53]\n"
        "s_memrealtime s[4:5]\n"
        "v_lshrrev_b32 v4, 6, v0\n"
        "v_and_b32 v1, 63, v0\n"
        "s_and_b32 s48, s2, 7\n"
        "v_readfirstlane_b32 s3, v4\n"
        "s_lshr_b32 s49, s2, 3\n"
        "v_lshrrev_b32 v2, 4, v1\n"
        "v_and_b32 v3, 15, v1\n"
        "s_mov_b32 s34, 1114112\n"
        "s_mov_b32 s35, 0x20000\n"
        "s_mov_b64 s[76:77], -1\n"
        "s_mov_b32 s7, 0\n"
        "s_and_b32 s50, s3, 3\n"
        "s_and_b32 s37, s3, 6\n"
        "s_cmp_lg_u32 s37, 2\n"
        "s_cbranch_scc1 .Lfb_PRB_X\n"
        "s_lshl_b32 s36, s2, 7\n"
        "s_and_b32 s37, s36, 0xffffe000\n"
        "s_add_u32 s36, s36, s37\n"
        "s_and_b32 s37, s3, 1\n"
        "s_lshl_b32 s38, s37, 13\n"
        "s_add_u32 s36, s36, s38\n"
        "s_add_u32 s36, s36, 1114112\n"
        "s_lshl_b32 s37, s37, 2\n"
        "s_add_u32 s37, s37, 135568\n"
        "v_mov_b32 v4, s37\n"
        "s_waitcnt lgkmcnt(0)\n"
        "s_memtime s[40:41]\n"
        "s_load_dword s39, s[30:31], s36\n"
        "s_waitcnt lgkmcnt(0)\n"
        "s_memtime s[42:43]\n"
        "s_waitcnt lgkmcnt(0)\n"
        "s_sub_u32 s36, s42, s40\n"
        "v_mov_b32 v5, s36\n"
        "ds_write_b32 v4, v5\n"
        ".Lfb_PRB_X:\n"
        "s_cmp_ge_u32 s49, 24\n"
        "s_cbranch_scc1 .Lfb_ROLE_F\n"
        "s_cmp_ge_u32 s49, 16\n"
        "s_cbranch_scc1 .Lfb_ROLE_Q\n"
        "s_cmp_lt_u32 s3, 4\n"
        "s_cbranch_scc1 .Lfb_A_C\n"
        "s_lshl_b32 s52, s50, 2\n"
        "v_xor_b32 v4, v3, v2\n"
        "v_xor_b32 v4, s52, v4\n"
        "v_lshlrev_b32 v5, 8, v2\n"
        "v_lshl_or_b32 v10, v4, 4, v5\n"
        "s_lshl_b32 s53, s50, 10\n"
        "s_lshl_b32 s54, s49, 12\n"
        "s_or_b32 s54, s54, s53\n"
        "s_lshl_b32 s55, s48, 24\n"
        "s_or_b32 s55, s55, 0xf00000\n"
        "s_mov_b32 s38, 0x100000\n"
        "s_mov_b32 s39, 0x20000\n"
        "s_add_u32 m0, s53, 135680\n"
        "s_lshl_b32 s59, s49, 2\n"
        "s_add_u32 s59, s59, s50\n"
        "s_and_b32 s51, s59, 1\n"
        "s_lshl_b32 s51, s51, 21\n"
        "s_lshr_b32 s59, s59, 1\n"
        "s_add_u32 s59, s59, 64\n"
        "s_lshl_b32 s59, s59, 13\n"
        "s_add_u32 s59, s59, s51\n"
        "s_lshl_b32 s51, s48, 10\n"
        "v_lshlrev_b32 v12, 4, v1\n"
        "s_mov_b32 s46, 0x400000\n"
        "s_mov_b32 s47, 0x20000\n"
        "s_waitcnt lgkmcnt(0)\n"
        "s_add_u32 s44, s14, s51\n"
        "s_addc_u32 s45, s15, 0\n"
        "buffer_load_dwordx4 v12, s[44:47], s59 offen lds\n"
        "s_add_u32 s59, s59, 262144\n"
        "buffer_load_dwordx4 v12, s[44:47], s59 offen lds\n"
        "s_add_u32 s59, s59, 262144\n"
        "buffer_load_dwordx4 v12, s[44:47], s59 offen lds\n"
        "s_add_u32 s59, s59, 262144\n"
        "buffer_load_dwordx4 v12, s[44:47], s59 offen lds\n"
        "s_add_u32 s59, s59, 262144\n"
        "buffer_load_dwordx4 v12, s[44:47], s59 offen lds\n"
        "s_add_u32 s59, s59, 262144\n"
        "buffer_load_dwordx4 v12, s[44:47], s59 offen lds\n"
        "s_add_u32 s59, s59, 262144\n"
        "s_mov_b32 m0, s53\n"
        "s_add_u32 s36, s10, s55\n"
        "s_addc_u32 s37, s11, 0\n"
        "s_xor_b32 s59, s54, 0x0\n"
        "buffer_load_dwordx4 v10, s[36:39], s59 offen nt lds\n"
        "s_add_u32 m0, m0, 4096\n"
        "s_xor_b32 s59, s54, 0x11000\n"
        "buffer_load_dwordx4 v10, s[36:39], s59 offen nt lds\n"
        "s_add_u32 m0, m0, 4096\n"
        "s_xor_b32 s59, s54, 0x22000\n"
        "buffer_load_dwordx4 v10, s[36:39], s59 offen nt lds\n"
        "s_add_u32 m0, m0, 4096\n"
        "s_xor_b32 s59, s54, 0x33000\n"
        "buffer_load_dwordx4 v10, s[36:39], s59 offen nt lds\n"
        "s_add_u32 m0, m0, 4096\n"
        "s_xor_b32 s59, s54, 0x44000\n"
        "buffer_load_dwordx4 v10, s[36:39], s59 offen nt lds\n"
        "s_add_u32 m0, m0, 4096\n"
        "s_add_u32 s40, s12, s55\n"
        "s_addc_u32 s41, s13, 0\n"
        "s_mov_b32 s42, 0x100000\n"
        "s_mov_b32 s43, 0x20000\n"
        "s_mov_b32 s32, s30\n"
        "s_mov_b32 s33, s31\n"
        "s_and_b32 s52, s52, 8\n"
        "v_lshlrev_b32 v4, 1, v2\n"
        "v_xor_b32 v4, v3, v4\n"
        "v_xor_b32 v4, s52, v4\n"
        "v_lshl_or_b32 v11, v4, 4, v5\n"
        "s_lshl_b32 s52, s48, 2\n"
        "s_add_u32 s52, s52, s50\n"
        "s_lshl_b32 s52, s52, 10\n"
        "s_and_b32 s51, s52, 0xffffe000\n"
        "s_add_u32 s52, s52, s51\n"
        "s_add_u32 s52, s52, 98304\n"
        "v_lshl_add_u32 v20, v1, 4, s52\n"
        "s_lshl_b32 s52, s50, 11\n"
        "s_add_u32 s52, s52, 8192\n"
        "v_lshl_add_u32 v21, v3, 7, s52\n"
        "s_mul_i32 s52, s50, 320\n"
        "s_add_u32 s52, s52, 131072\n"
        "v_lshl_add_u32 v22, v1, 2, s52\n"
        "s_and_b32 s52, s50, 1\n"
        "s_lshl_b32 s52, s52, 2\n"
        "v_lshrrev_b32 v4, 3, v1\n"
        "v_and_b32 v5, 2, v4\n"
        "v_or_b32 v5, s52, v5\n"
        "v_and_b32 v6, 7, v1\n"
        "v_xor_b32 v5, v6, v5\n"
        "v_mul_u32_u24 v4, 1024, v4\n"
        "v_lshl_add_u32 v23, v5, 4, v4\n"
        "s_lshr_b32 s52, s49, 3\n"
        "s_lshl_b32 s55, s48, 1\n"
        "s_add_u32 s52, s52, s55\n"
        "s_lshl_b32 s52, s52, 18\n"
        "s_and_b32 s55, s49, 7\n"
        "s_lshl_b32 s55, s55, 7\n"
        "s_add_u32 s52, s52, s55\n"
        "s_add_u32 s44, s16, s52\n"
        "s_addc_u32 s45, s17, 0\n"
        "s_mov_b32 s46, 0x40000\n"
        "s_mov_b32 s47, 0x20000\n"
        "s_lshl_b32 s52, s48, 11\n"
        "s_and_b32 s51, s52, 0xffffe000\n"
        "s_add_u32 s52, s52, s51\n"
        "s_add_u32 s52, s52, 786432\n"
        "v_lshlrev_b32 v4, 7, v3\n"
        "v_lshl_add_u32 v4, v2, 3, v4\n"
        "v_add_u32 v27, s52, v4\n"
        "s_lshl_b32 s75, s48, 7\n"
        "s_add_u32 s75, s75, 16384\n"
        "s_mov_b32 s70, 32768\n"
        "s_mov_b32 s73, 65536\n"
        "s_lshl_b32 s71, s50, 13\n"
        "s_xor_b32 s59, s54, 0x55000\n"
        "buffer_load_dwordx4 v10, s[36:39], s59 offen nt lds\n"
        "s_add_u32 m0, m0, 4096\n"
        "s_xor_b32 s59, s54, 0x66000\n"
        "buffer_load_dwordx4 v10, s[36:39], s59 offen nt lds\n"
        "s_add_u32 m0, m0, 4096\n"
        "s_xor_b32 s59, s54, 0x77000\n"
        "buffer_load_dwordx4 v10, s[36:39], s59 offen nt lds\n"
        "s_add_u32 m0, m0, 4096\n"
        "s_xor_b32 s59, s54, 0x88000\n"
        "buffer_load_dwordx4 v10, s[36:39], s59 offen nt lds\n"
        "s_add_u32 m0, m0, 4096\n"
        "s_xor_b32 s59, s54, 0x99000\n"
        "buffer_load_dwordx4 v10, s[36:39], s59 offen nt lds\n"
        "s_add_u32 m0, m0, 4096\n"
        "s_xor_b32 s59, s54, 0xaa000\n"
        "buffer_load_dwordx4 v10, s[36:39], s59 offen nt lds\n"
        "s_add_u32 m0, m0, 4096\n"
        "s_xor_b32 s59, s54, 0xbb000\n"
        "buffer_load_dwordx4 v10, s[36:39], s59 offen nt lds\n"
        "s_add_u32 m0, m0, 4096\n"
        "s_xor_b32 s59, s54, 0xcc000\n"
        "buffer_load_dwordx4 v10, s[36:39], s59 offen nt lds\n"
        "s_add_u32 m0, m0, 4096\n"
        "s_xor_b32 s59, s54, 0xdd000\n"
        "buffer_load_dwordx4 v10, s[36:39], s59 offen nt lds\n"
        "s_add_u32 m0, m0, 4096\n"
        "s_xor_b32 s59, s54, 0xee000\n"
        "buffer_load_dwordx4 v10, s[36:39], s59 offen nt lds\n"
        "s_add_u32 m0, m0, 4096\n"
        "s_xor_b32 s59, s54, 0xff000\n"
        "buffer_load_dwordx4 v10, s[36:39], s59 offen nt lds\n"
        "s_add_u32 m0, m0, 4096\n"
        "s_xor_b32 s59, s54, 0x0\n"
        "buffer_load_dwordx4 v11, s[40:43], s59 offen nt lds\n"
        "s_add_u32 m0, m0, 4096\n"
        "s_xor_b32 s59, s54, 0x11000\n"
        "buffer_load_dwordx4 v11, s[40:43], s59 offen nt lds\n"
        "s_add_u32 m0, m0, 4096\n"
        "s_xor_b32 s59, s54, 0x22000\n"
        "buffer_load_dwordx4 v11, s[40:43], s59 offen nt lds\n"
        "s_add_u32 m0, m0, 4096\n"
        "s_xor_b32 s59, s54, 0x33000\n"
        "buffer_load_dwordx4 v11, s[40:43], s59 offen nt lds\n"
        "s_add_u32 m0, m0, 4096\n"
        "s_xor_b32 s59, s54, 0x44000\n"
        "buffer_load_dwordx4 v11, s[40:43], s59 offen nt lds\n"
        "s_add_u32 m0, m0, 4096\n"
        "s_xor_b32 s59, s54, 0x55000\n"
        "buffer_load_dwordx4 v11, s[40:43], s59 offen nt lds\n"
        "s_add_u32 m0, m0, 4096\n"
        "s_xor_b32 s59, s54, 0x66000\n"
        "buffer_load_dwordx4 v11, s[40:43], s59 offen nt lds\n"
        "s_add_u32 m0, m0, 4096\n"
        "s_xor_b32 s59, s54, 0x77000\n"
        "buffer_load_dwordx4 v11, s[40:43], s59 offen nt lds\n"
        "s_add_u32 m0, m0, 4096\n"
        "s_xor_b32 s59, s54, 0x88000\n"
        "buffer_load_dwordx4 v11, s[40:43], s59 offen nt lds\n"
        "s_add_u32 m0, m0, 4096\n"
        "s_xor_b32 s59, s54, 0x99000\n"
        "buffer_load_dwordx4 v11, s[40:43], s59 offen nt lds\n"
        "s_add_u32 m0, m0, 4096\n"
        "s_xor_b32 s59, s54, 0xaa000\n"
        "buffer_load_dwordx4 v11, s[40:43], s59 offen nt lds\n"
        "s_add_u32 m0, m0, 4096\n"
        "s_xor_b32 s59, s54, 0xbb000\n"
        "buffer_load_dwordx4 v11, s[40:43], s59 offen nt lds\n"
        "s_add_u32 m0, m0, 4096\n"
        "s_xor_b32 s59, s54, 0xcc000\n"
        "buffer_load_dwordx4 v11, s[40:43], s59 offen nt lds\n"
        "s_add_u32 m0, m0, 4096\n"
        "s_xor_b32 s59, s54, 0xdd000\n"
        "buffer_load_dwordx4 v11, s[40:43], s59 offen nt lds\n"
        "s_add_u32 m0, m0, 4096\n"
        "s_xor_b32 s59, s54, 0xee000\n"
        "buffer_load_dwordx4 v11, s[40:43], s59 offen nt lds\n"
        "s_add_u32 m0, m0, 4096\n"
        "s_xor_b32 s59, s54, 0xff000\n"
        "buffer_load_dwordx4 v11, s[40:43], s59 offen nt lds\n"
        "s_add_u32 m0, m0, 4096\n"
        "s_waitcnt vmcnt(16)\n"
        "s_barrier\n"
        "v_mov_b32 v4, 135568\n"
        "ds_read_b64 v[4:5], v4\n"
        "s_waitcnt lgkmcnt(0)\n"
        "v_readfirstlane_b32 s51, v4\n"
        "v_readfirstlane_b32 s36, v5\n"
        "s_cmp_gt_u32 s51, s36\n"
        "s_cselect_b32 s51, 8192, 0\n"
        "s_add_u32 s36, s30, s51\n"
        "s_addc_u32 s37, s31, 0\n"
        "s_mov_b32 s38, 0x110000\n"
        "s_mov_b32 s39, 0x20000\n"
        "s_xor_b32 s51, s51, 8192\n"
        "s_add_u32 s40, s30, s51\n"
        "s_addc_u32 s41, s31, 0\n"
        "s_mov_b32 s42, 0x110000\n"
        "s_mov_b32 s43, 0x20000\n"
        "v_mov_b32 v8, 135552\n"
        "ds_read_b64 v[8:9], v8\n"
        "s_waitcnt lgkmcnt(0)\n"
        "v_readfirstlane_b32 s56, v8\n"
        "v_readfirstlane_b32 s57, v9\n"
        "s_waitcnt vmcnt(9)\n"
        "buffer_load_dwordx4 v[32:35], v20, s[36:39], 0 offen sc1\n"
        "buffer_load_dwordx4 v[36:39], v20, s[36:39], s73 offen sc1\n"
        "buffer_load_dwordx4 v[40:43], v21, s[32:35], 0 offen sc1\n"
        "s_barrier\n"
        "s_add_u32 m0, s53, 0\n"
        "s_add_u32 s59, s71, 0\n"
        "buffer_load_dwordx4 v23, s[44:47], s59 offen nt lds\n"
        "s_add_u32 m0, m0, 4096\n"
        "s_add_u32 s59, s59, s70\n"
        "buffer_load_dwordx4 v23, s[44:47], s59 offen nt lds\n"
        "s_add_u32 m0, m0, 4096\n"
        "s_add_u32 s59, s59, s70\n"
        "buffer_load_dwordx4 v23, s[44:47], s59 offen nt lds\n"
        "s_add_u32 m0, m0, 4096\n"
        "s_add_u32 s59, s59, s70\n"
        "buffer_load_dwordx4 v23, s[44:47], s59 offen nt lds\n"
        "s_add_u32 m0, m0, 4096\n"
        "s_add_u32 s59, s59, s70\n"
        "s_waitcnt vmcnt(5)\n"
        "v_add_f32 v44, v32, v36\n"
        "v_add_f32 v45, v34, v38\n"
        "v_xor_b32 v7, s56, v33\n"
        "v_bitop3_b32 v7, v35, s57, v7 bitop3:0xbe\n"
        "v_bitop3_b32 v7, v37, s56, v7 bitop3:0xbe\n"
        "v_bitop3_b32 v7, v39, s57, v7 bitop3:0xbe\n"
        "v_cvt_pk_bf16_f32 v44, v44, v45\n"
        "v_cmp_ne_u32 vcc, 0, v7\n"
        "s_cbranch_vccnz .Lfb_AL_Q\n"
        ".Lfb_AL_QOK:\n"
        "ds_write_b32 v22, v44\n"
        "s_cmp_lg_u32 s7, 0\n"
        "s_cbranch_scc1 .Lfb_DRYRET\n"
        "s_waitcnt lgkmcnt(0)\n"
        "s_barrier\n"
        "s_waitcnt vmcnt(4)\n"
        "v_xor_b32 v7, s56, v41\n"
        "v_bitop3_b32 v7, v43, s57, v7 bitop3:0xbe\n"
        "v_cmp_ne_u32 vcc, 0, v7\n"
        "s_cbranch_vccnz .Lfb_AL_F\n"
        ".Lfb_AL_FOK:\n"
        "s_add_u32 m0, s53, 16384\n"
        "s_add_u32 s59, s71, 131072\n"
        "buffer_load_dwordx4 v23, s[44:47], s59 offen nt lds\n"
        "s_add_u32 m0, m0, 4096\n"
        "s_add_u32 s59, s59, s70\n"
        "buffer_load_dwordx4 v23, s[44:47], s59 offen nt lds\n"
        "s_add_u32 m0, m0, 4096\n"
        "s_add_u32 s59, s59, s70\n"
        "buffer_load_dwordx4 v23, s[44:47], s59 offen nt lds\n"
        "s_add_u32 m0, m0, 4096\n"
        "s_add_u32 s59, s59, s70\n"
        "buffer_load_dwordx4 v23, s[44:47], s59 offen nt lds\n"
        "s_add_u32 m0, m0, 4096\n"
        "s_add_u32 s59, s59, s70\n"
        "s_barrier\n"
        "s_waitcnt vmcnt(0)\n"
        "s_cmp_lg_u32 s3, 4\n"
        "s_cbranch_scc1 .Lfb_AL_1\n"
        "s_mov_b32 s58, 65536\n"
        "buffer_load_dwordx2 v[40:41], v27, s[36:39], 0 offen sc1\n"
        "s_waitcnt vmcnt(0)\n"
        "s_branch .Lfb_AL_ML_c\n"
        ".Lfb_AL_ML_r:\n"
        "s_mov_b64 exec, vcc\n"
        "buffer_load_dwordx2 v[40:41], v27, s[36:39], 0 offen sc1\n"
        "s_mov_b64 exec, -1\n"
        "s_sub_u32 s58, s58, 1\n"
        "s_waitcnt vmcnt(0)\n"
        "s_cbranch_scc1 .Lfb_AL_ML_d\n"
        ".Lfb_AL_ML_c:\n"
        "v_max_u32 v7, v40, v41\n"
        "v_cmp_eq_u32 vcc, -1, v7\n"
        "s_cbranch_vccnz .Lfb_AL_ML_r\n"
        ".Lfb_AL_ML_d:\n"
        "s_barrier\n"
        "s_cmp_lg_u32 s49, 0\n"
        "s_cbranch_scc1 .Lfb_AL_2\n"
        "v_mov_b32 v44, v8\n"
        "v_mov_b32 v45, v8\n"
        "v_mov_b32 v46, v9\n"
        "v_mov_b32 v47, v9\n"
        "s_mov_b64 exec, 1\n"
        "buffer_store_dwordx4 v[44:47], off, s[40:43], s75 sc1\n"
        "buffer_store_dwordx4 v[44:47], off, s[36:39], s75 sc1\n"
        "s_branch .Lfb_AL_2\n"
        ".Lfb_AL_1:\n"
        "s_barrier\n"
        ".Lfb_AL_2:\n"
        "s_barrier\n"
        "s_endpgm\n"
        ".Lfb_A_C:\n"
        "s_cmp_lg_u32 s3, 0\n"
        "s_cbranch_scc1 .Lfb_AC_1\n"
        "s_waitcnt lgkmcnt(0)\n"
        "s_mov_b32 s32, s30\n"
        "s_mov_b32 s33, s31\n"
        "buffer_load_dwordx2 v[12:13], off, s[32:35], 0 sc1\n"
        "s_waitcnt vmcnt(0)\n"
        "v_readfirstlane_b32 s56, v12\n"
        "v_readfirstlane_b32 s57, v13\n"
        "s_add_u32 s56, s56, 1\n"
        "s_addc_u32 s57, s57, 0\n"
        "v_mov_b32 v12, s56\n"
        "v_mov_b32 v13, s57\n"
        "v_mov_b32 v9, 135552\n"
        "ds_write_b64 v9, v[12:13]\n"
        ".Lfb_AC_1:\n"
        "s_waitcnt lgkmcnt(0)\n"
        "s_mov_b32 s32, s30\n"
        "s_mov_b32 s33, s31\n"
        "s_lshl_b32 s52, s49, 3\n"
        "s_add_u32 s52, s52, s3\n"
        "s_and_b32 s52, s52, 0xff\n"
        "s_cmp_lg_u32 s52, 1\n"
        "s_cbranch_scc1 .Lfb_AC_TW\n"
        "s_sleep 2\n"
        "s_lshl_b32 s52, s48, 24\n"
        "s_or_b32 s52, s52, 0xf00000\n"
        "s_load_dword s36, s[12:13], s52\n"
        "s_add_u32 s52, s52, 0xffffc\n"
        "s_load_dword s37, s[12:13], s52\n"
        "s_lshl_b32 s52, s48, 19\n"
        "s_load_dword s38, s[16:17], s52\n"
        "s_add_u32 s52, s52, 0x7fffc\n"
        "s_load_dword s39, s[16:17], s52\n"
        "s_mov_b32 s52, 0x1bfffc\n"
        "s_load_dword s40, s[18:19], 0\n"
        "s_load_dword s41, s[18:19], s52\n"
        "s_load_dword s42, s[20:21], 0\n"
        "s_load_dword s43, s[20:21], s52\n"
        "s_load_dword s44, s[22:23], 0\n"
        "s_load_dword s45, s[22:23], s52\n"
        "s_load_dword s46, s[28:29], 0\n"
        ".Lfb_AC_TW:\n"
        "s_lshl_b32 s52, s50, 14\n"
        "v_lshl_add_u32 v5, v3, 8, s52\n"
        "v_or_b32 v4, 0, v2\n"
        "v_xor_b32 v4, v3, v4\n"
        "v_lshl_add_u32 v10, v4, 4, v5\n"
        "v_or_b32 v4, 4, v2\n"
        "v_xor_b32 v4, v3, v4\n"
        "v_lshl_add_u32 v11, v4, 4, v5\n"
        "v_or_b32 v4, 8, v2\n"
        "v_xor_b32 v4, v3, v4\n"
        "v_lshl_add_u32 v12, v4, 4, v5\n"
        "v_or_b32 v4, 12, v2\n"
        "v_xor_b32 v4, v3, v4\n"
        "v_lshl_add_u32 v13, v4, 4, v5\n"
        "s_lshl_b32 s52, s50, 2\n"
        "v_lshrrev_b32 v4, 2, v3\n"
        "v_lshl_add_u32 v5, v2, 3, v4\n"
        "v_lshlrev_b32 v5, 8, v5\n"
        "v_lshlrev_b32 v4, 1, v4\n"
        "v_and_b32 v6, 1, v2\n"
        "v_lshl_or_b32 v4, v6, 3, v4\n"
        "v_bfe_u32 v6, v1, 1, 1\n"
        "v_or_b32 v6, s52, v6\n"
        "v_xor_b32 v4, v6, v4\n"
        "v_lshl_add_u32 v5, v4, 4, v5\n"
        "v_and_b32 v6, 1, v1\n"
        "v_lshl_add_u32 v5, v6, 3, v5\n"
        "v_add_u32 v14, 65536, v5\n"
        "s_or_b32 s52, s52, 2\n"
        "v_lshrrev_b32 v4, 2, v3\n"
        "v_lshl_add_u32 v5, v2, 3, v4\n"
        "v_lshlrev_b32 v5, 8, v5\n"
        "v_lshlrev_b32 v4, 1, v4\n"
        "v_and_b32 v6, 1, v2\n"
        "v_lshl_or_b32 v4, v6, 3, v4\n"
        "v_bfe_u32 v6, v1, 1, 1\n"
        "v_or_b32 v6, s52, v6\n"
        "v_xor_b32 v4, v6, v4\n"
        "v_lshl_add_u32 v5, v4, 4, v5\n"
        "v_and_b32 v6, 1, v1\n"
        "v_lshl_add_u32 v5, v6, 3, v5\n"
        "v_add_u32 v15, 65536, v5\n"
        "v_and_b32 v6, 3, v1\n"
        "v_lshlrev_b32 v5, 4, v2\n"
        "v_mul_u32_u24 v4, 320, v6\n"
        "v_add_u32 v4, v4, v5\n"
        "v_add_u32 v4, 131072, v4\n"
        "v_xor_b32 v16, 32, v5\n"
        "v_add_u32 v16, 135296, v16\n"
        "s_mov_b32 s66, 0xf000f\n"
        "s_mov_b32 s67, 0xf000f\n"
        "v_cndmask_b32 v28, v16, v4, s[66:67]\n"
        "s_mov_b32 s66, 0xf000f0\n"
        "s_mov_b32 s67, 0xf000f0\n"
        "v_cndmask_b32 v29, v16, v4, s[66:67]\n"
        "s_mov_b32 s66, 0xf000f00\n"
        "s_mov_b32 s67, 0xf000f00\n"
        "v_cndmask_b32 v30, v16, v4, s[66:67]\n"
        "s_mov_b32 s66, 0xf000f000\n"
        "s_mov_b32 s67, 0xf000f000\n"
        "v_cndmask_b32 v31, v16, v4, s[66:67]\n"
        "v_mov_b32 v9, v4\n"
        "s_mov_b32 s72, 0xf000f00\n"
        "s_mov_b32 s73, 0xf000f00\n"
        "s_mov_b32 s74, 0xf000f000\n"
        "s_mov_b32 s75, 0xf000f000\n"
        "v_lshlrev_b32 v4, 2, v1\n"
        "v_add_u32 v4, 135296, v4\n"
        "v_mov_b32 v16, 0\n"
        "ds_write_b32 v4, v16\n"
        "v_mul_u32_u24 v4, 576, v6\n"
        "v_add_u32 v4, 132352, v4\n"
        "v_add_u32 v17, v4, v5\n"
        "s_lshl_b32 s52, s50, 7\n"
        "v_bfe_u32 v5, v1, 2, 2\n"
        "v_lshlrev_b32 v5, 5, v5\n"
        "v_lshl_add_u32 v5, v2, 3, v5\n"
        "v_add3_u32 v18, v4, v5, s52\n"
        "v_lshlrev_b32 v4, 5, v6\n"
        "v_add_u32 v19, 134656, v4\n"
        "s_lshl_b32 s52, s50, 2\n"
        "v_add_u32 v20, s52, v19\n"
        "s_lshl_b32 s52, s48, 16\n"
        "s_lshl_b32 s53, s49, 9\n"
        "s_add_u32 s52, s52, s53\n"
        "s_lshl_b32 s53, s50, 7\n"
        "s_add_u32 s52, s52, s53\n"
        "s_add_u32 s52, s52, 262144\n"
        "v_lshlrev_b32 v4, 14, v6\n"
        "v_lshl_add_u32 v4, v2, 4, v4\n"
        "v_and_b32 v5, 4, v1\n"
        "v_lshl_add_u32 v4, v5, 4, v4\n"
        "v_add_u32 v21, s52, v4\n"
        "s_lshl_b32 s55, s48, 11\n"
        "s_and_b32 s53, s55, 0xffffe000\n"
        "s_add_u32 s55, s55, s53\n"
        "s_add_u32 s55, s55, 786432\n"
        "s_lshl_b32 s53, s49, 7\n"
        "s_add_u32 s52, s55, s53\n"
        "v_lshl_add_u32 v22, v6, 3, s52\n"
        "s_lshl_b32 s52, s50, 1\n"
        "v_lshrrev_b32 v4, 2, v3\n"
        "v_lshl_add_u32 v5, v2, 3, v4\n"
        "v_lshlrev_b32 v5, 7, v5\n"
        "v_and_b32 v4, 2, v4\n"
        "v_and_b32 v6, 1, v2\n"
        "v_lshl_or_b32 v4, v6, 2, v4\n"
        "v_bfe_u32 v6, v1, 1, 1\n"
        "v_or_b32 v6, s52, v6\n"
        "v_xor_b32 v4, v6, v4\n"
        "v_lshl_add_u32 v5, v4, 4, v5\n"
        "v_and_b32 v6, 1, v1\n"
        "v_lshl_add_u32 v23, v6, 3, v5\n"
        "s_lshr_b32 s54, s49, 3\n"
        "s_lshl_b32 s53, s48, 1\n"
        "s_add_u32 s52, s54, s53\n"
        "s_lshl_b32 s52, s52, 11\n"
        "s_and_b32 s53, s52, 0xffffe000\n"
        "s_add_u32 s52, s52, s53\n"
        "s_and_b32 s53, s49, 7\n"
        "s_lshl_b32 s53, s53, 8\n"
        "s_add_u32 s52, s52, s53\n"
        "s_lshl_b32 s53, s50, 6\n"
        "s_add_u32 s52, s52, s53\n"
        "s_add_u32 s52, s52, 819200\n"
        "v_lshlrev_b32 v4, 4, v2\n"
        "v_add_u32 v16, s52, v4\n"
        "v_add_u32 v27, 134784, v4\n"
        "s_lshl_b32 s54, s54, 1\n"
        "s_lshr_b32 s53, s50, 1\n"
        "s_add_u32 s54, s54, s53\n"
        "s_lshl_b32 s53, s54, 3\n"
        "s_add_u32 s55, s55, s53\n"
        "v_lshl_add_u32 v25, v2, 7, s55\n"
        "s_lshl_b32 s53, s48, 2\n"
        "s_add_u32 s54, s54, s53\n"
        "s_lshl_b32 s54, s54, 14\n"
        "s_and_b32 s53, s50, 1\n"
        "s_lshl_b32 s53, s53, 8\n"
        "s_add_u32 s54, s54, s53\n"
        "s_add_u32 s54, s54, 262144\n"
        "v_lshlrev_b32 v4, 4, v3\n"
        "v_lshl_add_u32 v4, v2, 9, v4\n"
        "v_add_u32 v24, s54, v4\n"
        "s_lshl_b32 s53, s50, 7\n"
        "s_add_u32 s53, s53, 134784\n"
        "v_lshl_add_u32 v4, v3, 2, v2\n"
        "v_lshl_add_u32 v26, v4, 1, s53\n"
        "s_mov_b32 s60, 0xf000f0\n"
        "s_mov_b32 s61, 0xf000f0\n"
        "s_mov_b32 s64, 0xff00ff\n"
        "s_mov_b32 s65, 0xff00ff\n"
        "s_mov_b32 s66, 0x10001\n"
        "s_mov_b32 s67, 0x10001\n"
        "s_cmp_eq_u32 s3, 0\n"
        "s_cselect_b32 s62, 15, 0\n"
        "s_mov_b32 s63, 0\n"
        "s_mov_b32 s6, 0\n"
        "s_mov_b32 s68, 4096\n"
        "s_mov_b32 s69, 0x3fb8aa3b\n"
        "s_mov_b32 s70, 0x3db504f3\n"
        "s_waitcnt lgkmcnt(0)\n"
        "s_mov_b32 s7, 1\n"
        "s_mov_b64 s[76:77], 0\n"
        "s_mov_b64 exec, 0\n"
        "s_mov_b32 s34, 0\n"
        "s_mov_b32 s36, s30\n"
        "s_mov_b32 s37, s31\n"
        "s_mov_b32 s38, 0\n"
        "s_mov_b32 s39, 0x20000\n"
        "s_mov_b32 s40, s30\n"
        "s_mov_b32 s41, s31\n"
        "s_mov_b32 s42, 0\n"
        "s_mov_b32 s43, 0x20000\n"
        "s_cmp_lg_u32 s50, 0\n"
        "s_cbranch_scc1 .Lfb_AC_D_0\n"
        "s_call_b64 s[26:27], .Lfb_AC_S1\n"
        "s_branch .Lfb_AC_D_x\n"
        ".Lfb_AC_D_0:\n"
        "s_cmp_lg_u32 s50, 1\n"
        "s_cbranch_scc1 .Lfb_AC_D_1\n"
        "s_call_b64 s[26:27], .Lfb_AC_S2\n"
        "s_branch .Lfb_AC_D_x\n"
        ".Lfb_AC_D_1:\n"
        "s_cmp_lg_u32 s50, 2\n"
        "s_cbranch_scc1 .Lfb_AC_D_2\n"
        "s_call_b64 s[26:27], .Lfb_AC_S3\n"
        "s_call_b64 s[26:27], .Lfb_AL_Q\n"
        "s_branch .Lfb_AC_D_x\n"
        ".Lfb_AC_D_2:\n"
        "s_cmp_lg_u32 s50, 3\n"
        "s_cbranch_scc1 .Lfb_AC_D_3\n"
        "s_call_b64 s[26:27], .Lfb_AC_S4\n"
        "s_call_b64 s[26:27], .Lfb_AC_R\n"
        "s_branch .Lfb_AC_D_x\n"
        ".Lfb_AC_D_3:\n"
        ".Lfb_AC_D_x:\n"
        "s_waitcnt vmcnt(63) lgkmcnt(0)\n"
        "s_mov_b32 s7, 0\n"
        "s_mov_b32 s6, 0\n"
        "s_mov_b64 s[76:77], -1\n"
        "s_mov_b64 exec, -1\n"
        "s_mov_b32 s34, 1114112\n"
        "s_waitcnt lgkmcnt(0)\n"
        "s_barrier\n"
        "ds_read_b128 v[64:67], v10 offset:0\n"
        "ds_read_b128 v[68:71], v11 offset:0\n"
        "ds_read_b128 v[72:75], v12 offset:0\n"
        "ds_read_b128 v[76:79], v13 offset:0\n"
        "ds_read_b128 v[80:83], v10 offset:4096\n"
        "ds_read_b128 v[84:87], v11 offset:4096\n"
        "ds_read_b128 v[88:91], v12 offset:4096\n"
        "ds_read_b128 v[92:95], v13 offset:4096\n"
        "s_waitcnt lgkmcnt(4)\n"
        "ds_read_b128 v[96:99], v10 offset:8192\n"
        "ds_read_b128 v[100:103], v11 offset:8192\n"
        "ds_read_b128 v[104:107], v12 offset:8192\n"
        "ds_read_b128 v[108:111], v13 offset:8192\n"
        "ds_read_b128 v[112:115], v10 offset:12288\n"
        "ds_read_b128 v[116:119], v11 offset:12288\n"
        "ds_read_b128 v[120:123], v12 offset:12288\n"
        "ds_read_b128 v[124:127], v13 offset:12288\n"
        "s_waitcnt lgkmcnt(4)\n"
        "v_mov_b32 v4, 135568\n"
        "ds_read_b64 v[4:5], v4\n"
        "s_waitcnt lgkmcnt(0)\n"
        "v_readfirstlane_b32 s51, v4\n"
        "v_readfirstlane_b32 s36, v5\n"
        "s_cmp_gt_u32 s51, s36\n"
        "s_cselect_b32 s51, 8192, 0\n"
        "s_add_u32 s36, s30, s51\n"
        "s_addc_u32 s37, s31, 0\n"
        "s_mov_b32 s38, 0x110000\n"
        "s_mov_b32 s39, 0x20000\n"
        "s_xor_b32 s51, s51, 8192\n"
        "s_add_u32 s40, s30, s51\n"
        "s_addc_u32 s41, s31, 0\n"
        "s_mov_b32 s42, 0x110000\n"
        "s_mov_b32 s43, 0x20000\n"
        "s_waitcnt lgkmcnt(0)\n"
        "s_barrier\n"
        "ds_read_b64_tr_b16 v[128:129], v14 offset:0\n"
        "ds_read_b64_tr_b16 v[130:131], v14 offset:1024\n"
        "ds_read_b64_tr_b16 v[160:161], v15 offset:0\n"
        "ds_read_b64_tr_b16 v[162:163], v15 offset:1024\n"
        "s_waitcnt lgkmcnt(8)\n"
        "ds_read_b64_tr_b16 v[132:133], v14 offset:8192\n"
        "ds_read_b64_tr_b16 v[134:135], v14 offset:9216\n"
        "ds_read_b64_tr_b16 v[164:165], v15 offset:8192\n"
        "ds_read_b64_tr_b16 v[166:167], v15 offset:9216\n"
        "s_waitcnt lgkmcnt(8)\n"
        "ds_read_b64_tr_b16 v[136:137], v14 offset:16384\n"
        "ds_read_b64_tr_b16 v[138:139], v14 offset:17408\n"
        "ds_read_b64_tr_b16 v[168:169], v15 offset:16384\n"
        "ds_read_b64_tr_b16 v[170:171], v15 offset:17408\n"
        "s_waitcnt lgkmcnt(8)\n"
        "s_waitcnt lgkmcnt(0)\n"
        "s_barrier\n"
        ".Lfb_AC_S1:\n"
        "ds_read_b128 v[32:35], v9 offset:0\n"
        "ds_read_b128 v[36:39], v9 offset:64\n"
        "ds_read_b128 v[40:43], v9 offset:128\n"
        "ds_read_b128 v[44:47], v9 offset:192\n"
        "s_waitcnt lgkmcnt(3)\n"
        "v_mfma_f32_16x16x32_bf16 v[48:51], v[64:67], v[32:35], 0\n"
        "s_waitcnt lgkmcnt(2)\n"
        "v_mfma_f32_16x16x32_bf16 v[48:51], v[68:71], v[36:39], v[48:51]\n"
        "s_waitcnt lgkmcnt(1)\n"
        "v_mfma_f32_16x16x32_bf16 v[48:51], v[72:75], v[40:43], v[48:51]\n"
        "s_waitcnt lgkmcnt(0)\n"
        "v_mfma_f32_16x16x32_bf16 v[48:51], v[76:79], v[44:47], v[48:51]\n"
        "ds_read_b64_tr_b16 v[140:141], v14 offset:24576\n"
        "v_mfma_f32_16x16x32_bf16 v[52:55], v[80:83], v[32:35], 0\n"
        "ds_read_b64_tr_b16 v[142:143], v14 offset:25600\n"
        "v_mfma_f32_16x16x32_bf16 v[52:55], v[84:87], v[36:39], v[52:55]\n"
        "ds_read_b64_tr_b16 v[172:173], v15 offset:24576\n"
        "v_mfma_f32_16x16x32_bf16 v[52:55], v[88:91], v[40:43], v[52:55]\n"
        "ds_read_b64_tr_b16 v[174:175], v15 offset:25600\n"
        "v_mfma_f32_16x16x32_bf16 v[52:55], v[92:95], v[44:47], v[52:55]\n"
        "ds_read_b64_tr_b16 v[144:145], v14 offset:32768\n"
        "v_mfma_f32_16x16x32_bf16 v[56:59], v[96:99], v[32:35], 0\n"
        "ds_read_b64_tr_b16 v[146:147], v14 offset:33792\n"
        "v_mfma_f32_16x16x32_bf16 v[56:59], v[100:103], v[36:39], v[56:59]\n"
        "ds_read_b64_tr_b16 v[176:177], v15 offset:32768\n"
        "v_mfma_f32_16x16x32_bf16 v[56:59], v[104:107], v[40:43], v[56:59]\n"
        "ds_read_b64_tr_b16 v[178:179], v15 offset:33792\n"
        "v_mfma_f32_16x16x32_bf16 v[56:59], v[108:111], v[44:47], v[56:59]\n"
        "ds_read_b64_tr_b16 v[148:149], v14 offset:40960\n"
        "v_mfma_f32_16x16x32_bf16 v[60:63], v[112:115], v[32:35], 0\n"
        "ds_read_b64_tr_b16 v[150:151], v14 offset:41984\n"
        "v_cndmask_b32 v48, v48, v52, s[60:61]\n"
        "v_mfma_f32_16x16x32_bf16 v[60:63], v[116:119], v[36:39], v[60:63]\n"
        "ds_read_b64_tr_b16 v[180:181], v15 offset:40960\n"
        "v_cndmask_b32 v49, v49, v53, s[60:61]\n"
        "v_mfma_f32_16x16x32_bf16 v[60:63], v[120:123], v[40:43], v[60:63]\n"
        "ds_read_b64_tr_b16 v[182:183], v15 offset:41984\n"
        "v_cndmask_b32 v50, v50, v54, s[60:61]\n"
        "v_mfma_f32_16x16x32_bf16 v[60:63], v[124:127], v[44:47], v[60:63]\n"
        "ds_read_b64_tr_b16 v[152:153], v14 offset:49152\n"
        "v_cndmask_b32 v51, v51, v55, s[60:61]\n"
        "v_cndmask_b32 v48, v48, v56, s[72:73]\n"
        "v_cndmask_b32 v49, v49, v57, s[72:73]\n"
        "v_cndmask_b32 v50, v50, v58, s[72:73]\n"
        "v_cndmask_b32 v51, v51, v59, s[72:73]\n"
        "s_waitcnt lgkmcnt(4)\n"
        "ds_read_b64_tr_b16 v[154:155], v14 offset:50176\n"
        "ds_read_b64_tr_b16 v[184:185], v15 offset:49152\n"
        "ds_read_b64_tr_b16 v[186:187], v15 offset:50176\n"
        "v_cndmask_b32 v48, v48, v60, s[74:75]\n"
        "v_cndmask_b32 v49, v49, v61, s[74:75]\n"
        "v_cndmask_b32 v50, v50, v62, s[74:75]\n"
        "v_cndmask_b32 v51, v51, v63, s[74:75]\n"
        "v_cvt_pk_bf16_f32 v52, v48, v49\n"
        "v_cvt_pk_bf16_f32 v53, v50, v51\n"
        "v_lshlrev_b32 v48, 16, v52\n"
        "v_and_b32 v49, 0xffff0000, v52\n"
        "v_lshlrev_b32 v50, 16, v53\n"
        "v_and_b32 v51, 0xffff0000, v53\n"
        "v_pk_mul_f32 v[48:49], v[48:49], s[70:71] op_sel_hi:[1,0]\n"
        "v_pk_mul_f32 v[50:51], v[50:51], s[70:71] op_sel_hi:[1,0]\n"
        "v_cvt_pk_bf16_f32 v52, v48, v49\n"
        "v_cvt_pk_bf16_f32 v53, v50, v51\n"
        "v_lshlrev_b32 v48, 16, v52\n"
        "v_and_b32 v49, 0xffff0000, v52\n"
        "v_lshlrev_b32 v50, 16, v53\n"
        "v_and_b32 v51, 0xffff0000, v53\n"
        "v_max3_f32 v54, v48, v49, v50\n"
        "v_max_f32 v54, v54, v51\n"
        "ds_read_b64_tr_b16 v[156:157], v14 offset:57344\n"
        "ds_read_b64_tr_b16 v[158:159], v14 offset:58368\n"
        "v_max_f32_dpp v54, v54, v54 row_ror:4 row_mask:0xf bank_mask:0xf\n"
        "ds_read_b64_tr_b16 v[188:189], v15 offset:57344\n"
        "ds_read_b64_tr_b16 v[190:191], v15 offset:58368\n"
        "v_max_f32_dpp v54, v54, v54 row_ror:8 row_mask:0xf bank_mask:0xf\n"
        "v_mov_b32 v55, v54\n"
        "s_nop 1\n"
        "v_permlane16_swap_b32 v54, v55\n"
        "v_max_f32 v54, v54, v55\n"
        "v_mov_b32 v55, v54\n"
        "s_nop 1\n"
        "v_permlane32_swap_b32 v54, v55\n"
        "v_max_f32 v54, v54, v55\n"
        "v_mul_f32 v55, s69, v54\n"
        "v_fma_f32 v48, v48, s69, -v55\n"
        "v_fma_f32 v49, v49, s69, -v55\n"
        "v_fma_f32 v50, v50, s69, -v55\n"
        "v_fma_f32 v51, v51, s69, -v55\n"
        "v_exp_f32 v48, v48\n"
        "v_exp_f32 v49, v49\n"
        "v_exp_f32 v50, v50\n"
        "v_exp_f32 v51, v51\n"
        "v_cvt_pk_bf16_f32 v52, v48, v49\n"
        "v_add_f32 v48, v48, v49\n"
        "v_cvt_pk_bf16_f32 v53, v50, v51\n"
        "v_add_f32 v50, v50, v51\n"
        "ds_write_b64 v18, v[52:53]\n"
        "v_add_f32 v48, v48, v50\n"
        "s_nop 1\n"
        "v_add_f32_dpp v48, v48, v48 row_ror:4 row_mask:0xf bank_mask:0xf\n"
        "s_nop 1\n"
        "v_add_f32_dpp v48, v48, v48 row_ror:8 row_mask:0xf bank_mask:0xf\n"
        "v_mov_b32 v49, v48\n"
        "s_nop 1\n"
        "v_permlane16_swap_b32 v48, v49\n"
        "v_add_f32 v48, v48, v49\n"
        "v_mov_b32 v49, v48\n"
        "s_nop 1\n"
        "v_permlane32_swap_b32 v48, v49\n"
        "v_add_f32 v48, v48, v49\n"
        "ds_write2_b32 v20, v54, v48 offset1:4\n"
        "s_cmp_lg_u32 s7, 0\n"
        "s_cbranch_scc1 .Lfb_DRYRET\n"
        "s_waitcnt lgkmcnt(0)\n"
        "s_barrier\n"
        ".Lfb_AC_S2:\n"
        "ds_read_b128 v[192:195], v17\n"
        "ds_read_b128 v[224:227], v19\n"
        "ds_read_b128 v[228:231], v19 offset:16\n"
        "ds_read_b128 v[196:199], v17 offset:64\n"
        "ds_read_b128 v[200:203], v17 offset:128\n"
        "ds_read_b128 v[204:207], v17 offset:192\n"
        "ds_read_b128 v[208:211], v17 offset:256\n"
        "ds_read_b128 v[212:215], v17 offset:320\n"
        "ds_read_b128 v[216:219], v17 offset:384\n"
        "ds_read_b128 v[220:223], v17 offset:448\n"
        "s_waitcnt lgkmcnt(9)\n"
        "v_mfma_f32_16x16x32_bf16 v[64:67], v[128:131], v[192:195], 0\n"
        "v_mfma_f32_16x16x32_bf16 v[68:71], v[160:163], v[192:195], 0\n"
        "s_waitcnt lgkmcnt(7)\n"
        "v_max3_f32 v56, v224, v225, v226\n"
        "v_max_f32 v56, v56, v227\n"
        "s_waitcnt lgkmcnt(6)\n"
        "v_mfma_f32_16x16x32_bf16 v[64:67], v[132:135], v[196:199], v[64:67]\n"
        "v_mul_f32 v58, s69, v56\n"
        "v_fma_f32 v232, v224, s69, -v58\n"
        "v_mfma_f32_16x16x32_bf16 v[68:71], v[164:167], v[196:199], v[68:71]\n"
        "v_fma_f32 v234, v225, s69, -v58\n"
        "v_fma_f32 v236, v226, s69, -v58\n"
        "s_waitcnt lgkmcnt(5)\n"
        "v_mfma_f32_16x16x32_bf16 v[72:75], v[136:139], v[200:203], 0\n"
        "v_fma_f32 v238, v227, s69, -v58\n"
        "v_exp_f32 v232, v232\n"
        "v_mfma_f32_16x16x32_bf16 v[76:79], v[168:171], v[200:203], 0\n"
        "v_exp_f32 v234, v234\n"
        "v_exp_f32 v236, v236\n"
        "s_waitcnt lgkmcnt(4)\n"
        "v_mfma_f32_16x16x32_bf16 v[72:75], v[140:143], v[204:207], v[72:75]\n"
        "v_exp_f32 v238, v238\n"
        "v_mul_f32 v57, v232, v228\n"
        "v_mfma_f32_16x16x32_bf16 v[76:79], v[172:175], v[204:207], v[76:79]\n"
        "v_fmac_f32 v57, v234, v229\n"
        "v_fmac_f32 v57, v236, v230\n"
        "s_waitcnt lgkmcnt(3)\n"
        "v_mfma_f32_16x16x32_bf16 v[80:83], v[144:147], v[208:211], 0\n"
        "v_fmac_f32 v57, v238, v231\n"
        "s_and_b64 exec, s[62:63], s[76:77]\n"
        "buffer_store_dwordx2 v[56:57], v22, s[36:39], 0 offen sc1\n"
        "buffer_store_dwordx2 v[56:57], v22, s[40:43], 0 offen sc1\n"
        "s_mov_b64 exec, s[76:77]\n"
        "v_mul_f32 v48, v232, v64\n"
        "v_mfma_f32_16x16x32_bf16 v[84:87], v[176:179], v[208:211], 0\n"
        "v_mul_f32 v49, v232, v65\n"
        "v_mul_f32 v50, v232, v66\n"
        "s_waitcnt lgkmcnt(2)\n"
        "v_mfma_f32_16x16x32_bf16 v[80:83], v[148:151], v[212:215], v[80:83]\n"
        "v_mul_f32 v51, v232, v67\n"
        "v_mul_f32 v52, v232, v68\n"
        "v_mfma_f32_16x16x32_bf16 v[84:87], v[180:183], v[212:215], v[84:87]\n"
        "v_mul_f32 v53, v232, v69\n"
        "v_mul_f32 v54, v232, v70\n"
        "s_waitcnt lgkmcnt(1)\n"
        "v_mfma_f32_16x16x32_bf16 v[88:91], v[152:155], v[216:219], 0\n"
        "v_mul_f32 v55, v232, v71\n"
        "v_fmac_f32 v48, v234, v72\n"
        "v_mfma_f32_16x16x32_bf16 v[92:95], v[184:187], v[216:219], 0\n"
        "v_fmac_f32 v49, v234, v73\n"
        "v_fmac_f32 v50, v234, v74\n"
        "s_waitcnt lgkmcnt(0)\n"
        "v_mfma_f32_16x16x32_bf16 v[88:91], v[156:159], v[220:223], v[88:91]\n"
        "v_fmac_f32 v51, v234, v75\n"
        "v_fmac_f32 v52, v234, v76\n"
        "v_mfma_f32_16x16x32_bf16 v[92:95], v[188:191], v[220:223], v[92:95]\n"
        "v_fmac_f32 v53, v234, v77\n"
        "v_fmac_f32 v54, v234, v78\n"
        "v_fmac_f32 v55, v234, v79\n"
        "v_pk_fma_f32 v[48:49], v[236:237], v[80:81], v[48:49] op_sel_hi:[0,1,1]\n"
        "v_pk_fma_f32 v[50:51], v[236:237], v[82:83], v[50:51] op_sel_hi:[0,1,1]\n"
        "v_pk_fma_f32 v[52:53], v[236:237], v[84:85], v[52:53] op_sel_hi:[0,1,1]\n"
        "v_pk_fma_f32 v[54:55], v[236:237], v[86:87], v[54:55] op_sel_hi:[0,1,1]\n"
        "v_pk_fma_f32 v[48:49], v[238:239], v[88:89], v[48:49] op_sel_hi:[0,1,1]\n"
        "v_pk_fma_f32 v[50:51], v[238:239], v[90:91], v[50:51] op_sel_hi:[0,1,1]\n"
        "v_pk_fma_f32 v[52:53], v[238:239], v[92:93], v[52:53] op_sel_hi:[0,1,1]\n"
        "v_pk_fma_f32 v[54:55], v[238:239], v[94:95], v[54:55] op_sel_hi:[0,1,1]\n"
        "v_cndmask_b32 v48, v48, v52, s[60:61]\n"
        "v_cndmask_b32 v49, v49, v53, s[60:61]\n"
        "v_cndmask_b32 v50, v50, v54, s[60:61]\n"
        "v_cndmask_b32 v51, v51, v55, s[60:61]\n"
        "s_and_b64 exec, s[64:65], s[76:77]\n"
        "buffer_store_dwordx4 v[48:51], v21, s[36:39], 0 offen offset:0 sc1\n"
        "buffer_store_dwordx4 v[48:51], v21, s[40:43], 0 offen offset:0 sc1\n"
        "s_mov_b64 exec, s[76:77]\n"
        "s_cmp_lg_u32 s7, 0\n"
        "s_cbranch_scc1 .Lfb_DRYRET\n"
        "s_barrier\n"
        ".Lfb_AC_S3:\n"
        "buffer_load_dwordx2 v[208:209], v25, s[36:39], 0 offen offset:0 sc1\n"
        "buffer_load_dwordx2 v[210:211], v25, s[36:39], 0 offen offset:512 sc1\n"
        "buffer_load_dwordx2 v[212:213], v25, s[36:39], 0 offen offset:1024 sc1\n"
        "buffer_load_dwordx2 v[214:215], v25, s[36:39], 0 offen offset:1536 sc1\n"
        "buffer_load_dwordx4 v[32:35], v24, s[36:39], 0 offen offset:0 sc1\n"
        "buffer_load_dwordx4 v[36:39], v24, s[36:39], 0 offen offset:2048 sc1\n"
        "buffer_load_dwordx4 v[40:43], v24, s[36:39], s68 offen offset:0 sc1\n"
        "buffer_load_dwordx4 v[44:47], v24, s[36:39], s68 offen offset:2048 sc1\n"
        "ds_read_b64_tr_b16 v[96:97], v23 offset:0\n"
        "ds_read_b64_tr_b16 v[98:99], v23 offset:512\n"
        "ds_read_b64_tr_b16 v[100:101], v23 offset:4096\n"
        "ds_read_b64_tr_b16 v[102:103], v23 offset:4608\n"
        "ds_read_b64_tr_b16 v[104:105], v23 offset:8192\n"
        "ds_read_b64_tr_b16 v[106:107], v23 offset:8704\n"
        "ds_read_b64_tr_b16 v[108:109], v23 offset:12288\n"
        "ds_read_b64_tr_b16 v[110:111], v23 offset:12800\n"
        "ds_read_b64_tr_b16 v[112:113], v23 offset:16384\n"
        "ds_read_b64_tr_b16 v[114:115], v23 offset:16896\n"
        "ds_read_b64_tr_b16 v[116:117], v23 offset:20480\n"
        "ds_read_b64_tr_b16 v[118:119], v23 offset:20992\n"
        "s_waitcnt lgkmcnt(4)\n"
        "ds_read_b64_tr_b16 v[120:121], v23 offset:24576\n"
        "ds_read_b64_tr_b16 v[122:123], v23 offset:25088\n"
        "ds_read_b64_tr_b16 v[124:125], v23 offset:28672\n"
        "ds_read_b64_tr_b16 v[126:127], v23 offset:29184\n"
        "s_waitcnt vmcnt(4)\n"
        ".Lfb_AC_T2:\n"
        "v_max3_f32 v60, v208, v210, v212\n"
        "v_max_f32 v60, v60, v214\n"
        "v_mov_b32 v61, v60\n"
        "s_nop 1\n"
        "v_permlane16_swap_b32 v60, v61\n"
        "v_max_f32 v60, v60, v61\n"
        "v_mov_b32 v61, v60\n"
        "s_nop 1\n"
        "v_permlane32_swap_b32 v60, v61\n"
        "v_max_f32 v60, v60, v61\n"
        "v_mul_f32 v60, s69, v60\n"
        "v_fma_f32 v216, v208, s69, -v60\n"
        "v_fma_f32 v218, v210, s69, -v60\n"
        "v_fma_f32 v220, v212, s69, -v60\n"
        "v_fma_f32 v222, v214, s69, -v60\n"
        "v_exp_f32 v216, v216\n"
        "v_exp_f32 v218, v218\n"
        "v_exp_f32 v220, v220\n"
        "v_exp_f32 v222, v222\n"
        "v_mul_f32 v62, v216, v209\n"
        "v_fmac_f32 v62, v218, v211\n"
        "v_fmac_f32 v62, v220, v213\n"
        "v_fmac_f32 v62, v222, v215\n"
        "v_mov_b32 v63, v62\n"
        "s_nop 1\n"
        "v_permlane16_swap_b32 v62, v63\n"
        "v_add_f32 v62, v62, v63\n"
        "v_mov_b32 v63, v62\n"
        "s_nop 1\n"
        "v_permlane32_swap_b32 v62, v63\n"
        "v_add_f32 v62, v62, v63\n"
        "v_rcp_f32 v63, v62\n"
        "s_waitcnt vmcnt(0)\n"
        "v_pk_mul_f32 v[80:81], v[216:217], v[32:33] op_sel_hi:[0,1]\n"
        "v_pk_mul_f32 v[82:83], v[216:217], v[34:35] op_sel_hi:[0,1]\n"
        "v_pk_fma_f32 v[80:81], v[218:219], v[36:37], v[80:81] op_sel_hi:[0,1,1]\n"
        "v_pk_fma_f32 v[82:83], v[218:219], v[38:39], v[82:83] op_sel_hi:[0,1,1]\n"
        "v_pk_fma_f32 v[80:81], v[220:221], v[40:41], v[80:81] op_sel_hi:[0,1,1]\n"
        "v_pk_fma_f32 v[82:83], v[220:221], v[42:43], v[82:83] op_sel_hi:[0,1,1]\n"
        "v_pk_fma_f32 v[80:81], v[222:223], v[44:45], v[80:81] op_sel_hi:[0,1,1]\n"
        "v_pk_fma_f32 v[82:83], v[222:223], v[46:47], v[82:83] op_sel_hi:[0,1,1]\n"
        "s_nop 1\n"
        "v_permlane32_swap_b32 v80, v82\n"
        "v_permlane32_swap_b32 v81, v83\n"
        "v_add_f32 v80, v80, v82\n"
        "v_add_f32 v81, v81, v83\n"
        "s_nop 1\n"
        "v_permlane16_swap_b32 v80, v81\n"
        "v_add_f32 v80, v80, v81\n"
        "v_mul_f32 v81, v80, v63\n"
        "v_fma_f32 v82, -v81, v62, v80\n"
        "v_fma_f32 v81, v82, v63, v81\n"
        "v_cmp_u_f32 vcc, v81, v81\n"
        "v_cvt_pk_bf16_f32 v81, v81, v81\n"
        "s_cbranch_vccnz .Lfb_AC_R\n"
        ".Lfb_AC_C:\n"
        "ds_write_b16 v26, v81\n"
        "s_cmp_lg_u32 s7, 0\n"
        "s_cbranch_scc1 .Lfb_DRYRET\n"
        "s_waitcnt lgkmcnt(0)\n"
        "s_barrier\n"
        ".Lfb_AC_S4:\n"
        "ds_read_b128 v[192:195], v27 offset:0\n"
        "ds_read_b128 v[196:199], v27 offset:64\n"
        "ds_read_b128 v[200:203], v27 offset:128\n"
        "ds_read_b128 v[204:207], v27 offset:192\n"
        "ds_read_b128 v[208:211], v27 offset:256\n"
        "ds_read_b128 v[212:215], v27 offset:320\n"
        "ds_read_b128 v[216:219], v27 offset:384\n"
        "ds_read_b128 v[220:223], v27 offset:448\n"
        "s_waitcnt lgkmcnt(7)\n"
        "v_mfma_f32_16x16x32_bf16 v[48:51], v[96:99], v[192:195], 0\n"
        "s_waitcnt lgkmcnt(6)\n"
        "v_mfma_f32_16x16x32_bf16 v[48:51], v[100:103], v[196:199], v[48:51]\n"
        "s_waitcnt lgkmcnt(5)\n"
        "v_mfma_f32_16x16x32_bf16 v[48:51], v[104:107], v[200:203], v[48:51]\n"
        "s_waitcnt lgkmcnt(4)\n"
        "v_mfma_f32_16x16x32_bf16 v[48:51], v[108:111], v[204:207], v[48:51]\n"
        "s_waitcnt lgkmcnt(3)\n"
        "v_mfma_f32_16x16x32_bf16 v[48:51], v[112:115], v[208:211], v[48:51]\n"
        "s_waitcnt lgkmcnt(2)\n"
        "v_mfma_f32_16x16x32_bf16 v[48:51], v[116:119], v[212:215], v[48:51]\n"
        "s_waitcnt lgkmcnt(1)\n"
        "v_mfma_f32_16x16x32_bf16 v[48:51], v[120:123], v[216:219], v[48:51]\n"
        "s_waitcnt lgkmcnt(0)\n"
        "v_mfma_f32_16x16x32_bf16 v[48:51], v[124:127], v[220:223], v[48:51]\n"
        "s_and_b64 exec, s[66:67], s[76:77]\n"
        "s_nop 6\n"
        "buffer_store_dwordx4 v[48:51], v16, s[40:43], 0 offen offset:0 sc1\n"
        "buffer_store_dwordx4 v[48:51], v16, s[36:39], 0 offen offset:0 sc1\n"
        "s_cmp_lg_u32 s7, 0\n"
        "s_cbranch_scc1 .Lfb_DRYRET\n"
        "s_endpgm\n"
        ".Lfb_ROLE_Q:\n"
        "s_sub_u32 s49, s49, 16\n"
        "s_cmp_lt_u32 s3, 4\n"
        "s_cbranch_scc1 .Lfb_Q_C\n"
        "s_and_b32 s51, s50, 2\n"
        "s_lshl_b32 s51, s51, 2\n"
        "v_lshlrev_b32 v4, 1, v2\n"
        "v_xor_b32 v4, v3, v4\n"
        "v_xor_b32 v4, s51, v4\n"
        "v_mul_u32_u24 v5, 8192, v2\n"
        "v_lshl_add_u32 v10, v4, 4, v5\n"
        "s_and_b32 s52, s49, 1\n"
        "s_lshl_b32 s52, s52, 21\n"
        "s_lshr_b32 s53, s49, 1\n"
        "s_lshl_b32 s54, s48, 2\n"
        "s_add_u32 s53, s53, s54\n"
        "s_lshl_b32 s53, s53, 8\n"
        "s_add_u32 s52, s52, s53\n"
        "s_lshl_b32 s53, s50, 10\n"
        "s_mov_b32 m0, s53\n"
        "s_lshl_b32 s59, s50, 15\n"
        "s_mov_b32 s68, 131072\n"
        "s_waitcnt lgkmcnt(0)\n"
        "s_add_u32 s36, s14, s52\n"
        "s_addc_u32 s37, s15, 0\n"
        "s_mov_b32 s38, 0x200000\n"
        "s_mov_b32 s39, 0x20000\n"
        "buffer_load_dwordx4 v10, s[36:39], s59 offen lds\n"
        "s_add_u32 m0, m0, 4096\n"
        "s_add_u32 s59, s59, s68\n"
        "buffer_load_dwordx4 v10, s[36:39], s59 offen lds\n"
        "s_add_u32 m0, m0, 4096\n"
        "s_add_u32 s59, s59, s68\n"
        "buffer_load_dwordx4 v10, s[36:39], s59 offen lds\n"
        "s_add_u32 m0, m0, 4096\n"
        "s_add_u32 s59, s59, s68\n"
        "buffer_load_dwordx4 v10, s[36:39], s59 offen lds\n"
        "s_add_u32 m0, m0, 4096\n"
        "s_add_u32 s59, s59, s68\n"
        "buffer_load_dwordx4 v10, s[36:39], s59 offen lds\n"
        "s_add_u32 m0, m0, 4096\n"
        "s_add_u32 s59, s59, s68\n"
        "buffer_load_dwordx4 v10, s[36:39], s59 offen lds\n"
        "s_add_u32 m0, m0, 4096\n"
        "s_add_u32 s59, s59, s68\n"
        "buffer_load_dwordx4 v10, s[36:39], s59 offen lds\n"
        "s_add_u32 m0, m0, 4096\n"
        "s_add_u32 s59, s59, s68\n"
        "buffer_load_dwordx4 v10, s[36:39], s59 offen lds\n"
        "s_add_u32 m0, m0, 4096\n"
        "s_add_u32 s59, s59, s68\n"
        "buffer_load_dwordx4 v10, s[36:39], s59 offen lds\n"
        "s_add_u32 m0, m0, 4096\n"
        "s_add_u32 s59, s59, s68\n"
        "buffer_load_dwordx4 v10, s[36:39], s59 offen lds\n"
        "s_add_u32 m0, m0, 4096\n"
        "s_add_u32 s59, s59, s68\n"
        "buffer_load_dwordx4 v10, s[36:39], s59 offen lds\n"
        "s_add_u32 m0, m0, 4096\n"
        "s_add_u32 s59, s59, s68\n"
        "buffer_load_dwordx4 v10, s[36:39], s59 offen lds\n"
        "s_add_u32 m0, m0, 4096\n"
        "s_add_u32 s59, s59, s68\n"
        "buffer_load_dwordx4 v10, s[36:39], s59 offen lds\n"
        "s_add_u32 m0, m0, 4096\n"
        "s_add_u32 s59, s59, s68\n"
        "buffer_load_dwordx4 v10, s[36:39], s59 offen lds\n"
        "s_add_u32 m0, m0, 4096\n"
        "s_add_u32 s59, s59, s68\n"
        "buffer_load_dwordx4 v10, s[36:39], s59 offen lds\n"
        "s_add_u32 m0, m0, 4096\n"
        "s_add_u32 s59, s59, s68\n"
        "buffer_load_dwordx4 v10, s[36:39], s59 offen lds\n"
        "s_add_u32 m0, m0, 4096\n"
        "s_add_u32 s59, s59, s68\n"
        "s_mov_b32 s32, s30\n"
        "s_mov_b32 s33, s31\n"
        "s_mov_b64 s[28:29], s[26:27]\n"
        "s_waitcnt lgkmcnt(0)\n"
        "s_mov_b32 s7, 1\n"
        "s_mov_b64 s[76:77], 0\n"
        "s_mov_b64 exec, 0\n"
        "s_mov_b32 s34, 0\n"
        "s_mov_b32 s36, s30\n"
        "s_mov_b32 s37, s31\n"
        "s_mov_b32 s38, 0\n"
        "s_mov_b32 s39, 0x20000\n"
        "s_mov_b32 s40, s30\n"
        "s_mov_b32 s41, s31\n"
        "s_mov_b32 s42, 0\n"
        "s_mov_b32 s43, 0x20000\n"
        "s_cmp_lg_u32 s50, 0\n"
        "s_cbranch_scc1 .Lfb_QL_D_0\n"
        "s_call_b64 s[26:27], .Lfb_QC_S1\n"
        "s_call_b64 s[26:27], .Lfb_Q_H1_c\n"
        "s_branch .Lfb_QL_D_x\n"
        ".Lfb_QL_D_0:\n"
        "s_cmp_lg_u32 s50, 1\n"
        "s_cbranch_scc1 .Lfb_QL_D_1\n"
        "s_call_b64 s[26:27], .Lfb_QC_S2\n"
        "s_branch .Lfb_QL_D_x\n"
        ".Lfb_QL_D_1:\n"
        "s_cmp_lg_u32 s50, 2\n"
        "s_cbranch_scc1 .Lfb_QL_D_2\n"
        "s_call_b64 s[26:27], .Lfb_QC_A2\n"
        "s_branch .Lfb_QL_D_x\n"
        ".Lfb_QL_D_2:\n"
        "s_cmp_lg_u32 s50, 3\n"
        "s_cbranch_scc1 .Lfb_QL_D_3\n"
        "s_call_b64 s[26:27], .Lfb_QC_B\n"
        "s_branch .Lfb_QL_D_x\n"
        ".Lfb_QL_D_3:\n"
        ".Lfb_QL_D_x:\n"
        "s_waitcnt vmcnt(63) lgkmcnt(0)\n"
        "s_mov_b32 s7, 0\n"
        "s_mov_b32 s6, 0\n"
        "s_mov_b64 s[76:77], -1\n"
        "s_mov_b64 exec, -1\n"
        "s_mov_b32 s34, 1114112\n"
        "s_lshl_b32 s53, s50, 10\n"
        "s_and_b32 s51, s50, 2\n"
        "s_lshl_b32 s51, s51, 2\n"
        "v_lshlrev_b32 v4, 1, v2\n"
        "v_xor_b32 v4, v3, v4\n"
        "v_xor_b32 v4, s51, v4\n"
        "v_mul_u32_u24 v5, 6144, v2\n"
        "v_lshl_add_u32 v11, v4, 4, v5\n"
        "s_and_b32 s52, s49, 3\n"
        "s_mul_i32 s52, s52, 786432\n"
        "s_lshr_b32 s54, s49, 2\n"
        "s_lshl_b32 s55, s48, 1\n"
        "s_add_u32 s54, s54, s55\n"
        "s_lshl_b32 s54, s54, 8\n"
        "s_add_u32 s52, s52, s54\n"
        "s_add_u32 s52, s52, 2048\n"
        "s_add_u32 s40, s28, s52\n"
        "s_addc_u32 s41, s29, 0\n"
        "s_mov_b32 s42, 0xc0000\n"
        "s_mov_b32 s43, 0x20000\n"
        "s_waitcnt vmcnt(0)\n"
        "s_barrier\n"
        "s_barrier\n"
        "s_barrier\n"
        "s_add_u32 m0, s53, 65536\n"
        "s_mul_i32 s59, s50, 24576\n"
        "s_mov_b32 s68, 98304\n"
        "buffer_load_dwordx4 v11, s[40:43], s59 offen nt lds\n"
        "s_add_u32 m0, m0, 4096\n"
        "s_add_u32 s59, s59, s68\n"
        "buffer_load_dwordx4 v11, s[40:43], s59 offen nt lds\n"
        "s_add_u32 m0, m0, 4096\n"
        "s_add_u32 s59, s59, s68\n"
        "buffer_load_dwordx4 v11, s[40:43], s59 offen nt lds\n"
        "s_add_u32 m0, m0, 4096\n"
        "s_add_u32 s59, s59, s68\n"
        "buffer_load_dwordx4 v11, s[40:43], s59 offen nt lds\n"
        "s_add_u32 m0, m0, 4096\n"
        "s_add_u32 s59, s59, s68\n"
        "buffer_load_dwordx4 v11, s[40:43], s59 offen nt lds\n"
        "s_add_u32 m0, m0, 4096\n"
        "s_add_u32 s59, s59, s68\n"
        "buffer_load_dwordx4 v11, s[40:43], s59 offen nt lds\n"
        "s_add_u32 m0, m0, 4096\n"
        "s_add_u32 s59, s59, s68\n"
        "buffer_load_dwordx4 v11, s[40:43], s59 offen nt lds\n"
        "s_add_u32 m0, m0, 4096\n"
        "s_add_u32 s59, s59, s68\n"
        "buffer_load_dwordx4 v11, s[40:43], s59 offen nt lds\n"
        "s_add_u32 m0, m0, 4096\n"
        "s_add_u32 s59, s59, s68\n"
        "s_waitcnt vmcnt(0)\n"
        "s_barrier\n"
        "s_endpgm\n"
        ".Lfb_Q_C:\n"
        "s_and_b32 s52, s49, 1\n"
        "s_lshl_b32 s52, s52, 2\n"
        "s_add_u32 s52, s52, s3\n"
        "s_lshl_b32 s54, s52, 7\n"
        "v_lshlrev_b32 v4, 4, v1\n"
        "v_and_b32 v6, 31, v1\n"
        "v_lshl_add_u32 v5, v6, 2, s54\n"
        "s_and_b32 s53, s49, 3\n"
        "s_lshl_b32 s53, s53, 8\n"
        "v_lshl_add_u32 v7, v1, 2, s53\n"
        "s_waitcnt lgkmcnt(0)\n"
        "s_mov_b32 s32, s30\n"
        "s_mov_b32 s33, s31\n"
        "buffer_load_dwordx2 v[20:21], off, s[32:35], 0 sc1\n"
        "global_load_dwordx4 v[16:19], v4, s[8:9]\n"
        "global_load_dword v22, v5, s[8:9]\n"
        "global_load_dword v12, v7, s[24:25]\n"
        "s_lshl_b32 s52, s52, 9\n"
        "s_add_u32 s52, s52, 32768\n"
        "v_lshl_add_u32 v28, v6, 4, s52\n"
        "v_lshrrev_b32 v5, 5, v1\n"
        "v_lshl_add_u32 v28, v5, 12, v28\n"
        "s_lshl_b32 s52, s3, 7\n"
        "s_add_u32 s52, s52, 131072\n"
        "v_lshl_add_u32 v29, v6, 2, s52\n"
        "s_waitcnt vmcnt(0)\n"
        "v_readfirstlane_b32 s56, v20\n"
        "v_readfirstlane_b32 s57, v21\n"
        "s_add_u32 s56, s56, 1\n"
        "s_addc_u32 s57, s57, 0\n"
        "v_lshlrev_b32 v32, 16, v16\n"
        "v_and_b32 v33, 0xffff0000, v16\n"
        "v_lshlrev_b32 v34, 16, v17\n"
        "v_and_b32 v35, 0xffff0000, v17\n"
        "v_lshlrev_b32 v36, 16, v18\n"
        "v_and_b32 v37, 0xffff0000, v18\n"
        "v_lshlrev_b32 v38, 16, v19\n"
        "v_and_b32 v39, 0xffff0000, v19\n"
        "v_add_f32 v40, v32, v33\n"
        "v_add_f32 v40, v40, v34\n"
        "v_add_f32 v40, v40, v35\n"
        "v_add_f32 v40, v40, v36\n"
        "v_add_f32 v40, v40, v37\n"
        "v_add_f32 v40, v40, v38\n"
        "v_add_f32 v40, v40, v39\n"
        "s_nop 1\n"
        "v_add_f32_dpp v40, v40, v40 quad_perm:[1,0,3,2] row_mask:0xf bank_mask:0xf\n"
        "s_nop 1\n"
        "v_add_f32_dpp v40, v40, v40 quad_perm:[2,3,0,1] row_mask:0xf bank_mask:0xf\n"
        "s_nop 1\n"
        "v_add_f32_dpp v40, v40, v40 row_ror:4 row_mask:0xf bank_mask:0xf\n"
        "s_nop 1\n"
        "v_add_f32_dpp v40, v40, v40 row_ror:8 row_mask:0xf bank_mask:0xf\n"
        "v_mov_b32 v41, v40\n"
        "s_nop 1\n"
        "v_permlane16_swap_b32 v40, v41\n"
        "v_add_f32 v40, v40, v41\n"
        "v_mov_b32 v41, v40\n"
        "s_nop 1\n"
        "v_permlane32_swap_b32 v40, v41\n"
        "v_add_f32 v40, v40, v41\n"
        ".Lfb_QC_A2:\n"
        "v_mul_f32 v40, 0x3b000000, v40\n"
        "v_sub_f32 v32, v32, v40\n"
        "v_sub_f32 v33, v33, v40\n"
        "v_sub_f32 v34, v34, v40\n"
        "v_sub_f32 v35, v35, v40\n"
        "v_sub_f32 v36, v36, v40\n"
        "v_sub_f32 v37, v37, v40\n"
        "v_sub_f32 v38, v38, v40\n"
        "v_sub_f32 v39, v39, v40\n"
        "v_mul_f32 v42, v32, v32\n"
        "v_fmac_f32 v42, v33, v33\n"
        "v_fmac_f32 v42, v34, v34\n"
        "v_fmac_f32 v42, v35, v35\n"
        "v_fmac_f32 v42, v36, v36\n"
        "v_fmac_f32 v42, v37, v37\n"
        "v_fmac_f32 v42, v38, v38\n"
        "v_fmac_f32 v42, v39, v39\n"
        "s_nop 1\n"
        "v_add_f32_dpp v42, v42, v42 quad_perm:[1,0,3,2] row_mask:0xf bank_mask:0xf\n"
        "s_nop 1\n"
        "v_add_f32_dpp v42, v42, v42 quad_perm:[2,3,0,1] row_mask:0xf bank_mask:0xf\n"
        "s_nop 1\n"
        "v_add_f32_dpp v42, v42, v42 row_ror:4 row_mask:0xf bank_mask:0xf\n"
        "s_nop 1\n"
        "v_add_f32_dpp v42, v42, v42 row_ror:8 row_mask:0xf bank_mask:0xf\n"
        "v_mov_b32 v41, v42\n"
        "s_nop 1\n"
        "v_permlane16_swap_b32 v42, v41\n"
        "v_add_f32 v42, v42, v41\n"
        "v_mov_b32 v41, v42\n"
        "s_nop 1\n"
        "v_permlane32_swap_b32 v42, v41\n"
        "v_add_f32 v42, v42, v41\n"
        "v_mul_f32 v42, 0x3b000000, v42\n"
        "v_cvt_pk_bf16_f32 v42, v42, v42\n"
        "v_lshlrev_b32 v42, 16, v42\n"
        "v_add_f32 v42, 0x3727c5ac, v42\n"
        "v_cvt_pk_bf16_f32 v42, v42, v42\n"
        "v_lshlrev_b32 v42, 16, v42\n"
        "v_sqrt_f32 v42, v42\n"
        "s_nop 0\n"
        "v_cvt_pk_bf16_f32 v42, v42, v42\n"
        "v_lshlrev_b32 v42, 16, v42\n"
        "v_cvt_pk_bf16_f32 v40, v40, v40\n"
        "v_lshlrev_b32 v40, 16, v40\n"
        "v_rcp_f32 v43, v42\n"
        "v_lshlrev_b32 v32, 16, v22\n"
        "v_and_b32 v33, 0xffff0000, v22\n"
        "v_sub_f32 v32, v32, v40\n"
        "v_sub_f32 v33, v33, v40\n"
        "v_cvt_pk_bf16_f32 v34, v32, v33\n"
        "v_lshlrev_b32 v32, 16, v34\n"
        "v_and_b32 v33, 0xffff0000, v34\n"
        "v_mul_f32 v30, v32, v43\n"
        "v_fma_f32 v34, -v30, v42, v32\n"
        "v_fma_f32 v30, v34, v43, v30\n"
        "v_mul_f32 v31, v33, v43\n"
        "v_fma_f32 v34, -v31, v42, v33\n"
        "v_fma_f32 v31, v34, v43, v31\n"
        "v_cvt_pk_bf16_f32 v34, v30, v31\n"
        "v_lshlrev_b32 v30, 16, v34\n"
        "v_and_b32 v31, 0xffff0000, v34\n"
        "s_cmp_lg_u32 s7, 0\n"
        "s_cbranch_scc1 .Lfb_DRYRET\n"
        ".Lfb_QC_B:\n"
        "v_lshlrev_b32 v13, 16, v12\n"
        "v_and_b32 v14, 0xffff0000, v12\n"
        "v_mul_f32 v15, 0xbfb8aa3b, v13\n"
        "v_mul_f32 v16, 0xbfb8aa3b, v14\n"
        "v_exp_f32 v15, v15\n"
        "v_exp_f32 v16, v16\n"
        "v_add_f32 v15, 1.0, v15\n"
        "v_add_f32 v16, 1.0, v16\n"
        "v_rcp_f32 v15, v15\n"
        "v_rcp_f32 v16, v16\n"
        "v_mul_f32 v13, v13, v15\n"
        "v_mul_f32 v14, v14, v16\n"
        "v_cvt_pk_bf16_f32 v13, v13, v14\n"
        "v_lshlrev_b32 v14, 2, v1\n"
        "v_add_u32 v14, 134336, v14\n"
        "ds_write_b32 v14, v13\n"
        "s_lshl_b32 s52, s50, 2\n"
        "v_lshrrev_b32 v4, 2, v3\n"
        "v_lshl_add_u32 v5, v2, 3, v4\n"
        "v_lshlrev_b32 v5, 8, v5\n"
        "v_lshlrev_b32 v4, 1, v4\n"
        "v_and_b32 v6, 1, v2\n"
        "v_lshl_or_b32 v4, v6, 3, v4\n"
        "v_bfe_u32 v6, v1, 1, 1\n"
        "v_or_b32 v6, s52, v6\n"
        "v_xor_b32 v4, v6, v4\n"
        "v_lshl_add_u32 v5, v4, 4, v5\n"
        "v_and_b32 v6, 1, v1\n"
        "v_lshl_add_u32 v5, v6, 3, v5\n"
        "v_add_u32 v10, 0, v5\n"
        "s_or_b32 s52, s52, 2\n"
        "v_lshrrev_b32 v4, 2, v3\n"
        "v_lshl_add_u32 v5, v2, 3, v4\n"
        "v_lshlrev_b32 v5, 8, v5\n"
        "v_lshlrev_b32 v4, 1, v4\n"
        "v_and_b32 v6, 1, v2\n"
        "v_lshl_or_b32 v4, v6, 3, v4\n"
        "v_bfe_u32 v6, v1, 1, 1\n"
        "v_or_b32 v6, s52, v6\n"
        "v_xor_b32 v4, v6, v4\n"
        "v_lshl_add_u32 v5, v4, 4, v5\n"
        "v_and_b32 v6, 1, v1\n"
        "v_lshl_add_u32 v5, v6, 3, v5\n"
        "v_add_u32 v11, 0, v5\n"
        "v_add_u32 v20, 65536, v10\n"
        "v_add_u32 v21, 65536, v11\n"
        "v_lshlrev_b32 v4, 4, v2\n"
        "v_add_u32 v16, 131072, v4\n"
        "v_add_u32 v22, 134336, v4\n"
        "s_mov_b32 s60, 0xaaaaaaaa\n"
        "s_mov_b32 s61, 0xaaaaaaaa\n"
        "s_mov_b32 s66, 0x30003\n"
        "s_mov_b32 s67, 0x30003\n"
        "s_and_b32 s52, s49, 1\n"
        "s_lshl_b32 s53, s52, 16\n"
        "s_lshr_b32 s54, s49, 1\n"
        "s_lshl_b32 s55, s48, 2\n"
        "s_add_u32 s54, s54, s55\n"
        "s_lshl_b32 s54, s54, 10\n"
        "s_and_b32 s55, s54, 0xffffe000\n"
        "s_add_u32 s54, s54, s55\n"
        "s_add_u32 s53, s53, s54\n"
        "s_lshl_b32 s54, s50, 8\n"
        "s_add_u32 s53, s53, s54\n"
        "s_add_u32 s53, s53, 98304\n"
        "v_and_b32 v5, 1, v1\n"
        "v_lshl_add_u32 v6, v2, 1, v5\n"
        "v_lshl_add_u32 v17, v6, 4, s53\n"
        "s_and_b32 s52, s49, 3\n"
        "s_lshl_b32 s52, s52, 13\n"
        "s_lshr_b32 s54, s49, 2\n"
        "s_lshl_b32 s55, s48, 1\n"
        "s_add_u32 s54, s54, s55\n"
        "s_lshl_b32 s54, s54, 9\n"
        "s_add_u32 s52, s52, s54\n"
        "s_lshl_b32 s54, s50, 7\n"
        "s_add_u32 s52, s52, s54\n"
        "s_add_u32 s52, s52, 229376\n"
        "v_lshlrev_b32 v6, 4, v2\n"
        "v_lshl_add_u32 v6, v5, 6, v6\n"
        "v_add_u32 v23, s52, v6\n"
        "s_mov_b32 s68, 16384\n"
        "s_mov_b32 s69, 32768\n"
        "s_mov_b32 s70, 49152\n"
        "v_mov_b32 v57, s56\n"
        "v_mov_b32 v59, s57\n"
        "v_mov_b32 v61, s56\n"
        "v_mov_b32 v63, s57\n"
        "s_cmp_lg_u32 s7, 0\n"
        "s_cbranch_scc1 .Lfb_DRYRET\n"
        "s_waitcnt lgkmcnt(0)\n"
        "s_barrier\n"
        ".Lfb_QC_S1:\n"
        "v_mov_b32 v4, 135568\n"
        "ds_read_b64 v[4:5], v4\n"
        "s_waitcnt lgkmcnt(0)\n"
        "v_readfirstlane_b32 s51, v4\n"
        "v_readfirstlane_b32 s36, v5\n"
        "s_cmp_gt_u32 s51, s36\n"
        "s_cselect_b32 s51, 8192, 0\n"
        "s_add_u32 s36, s30, s51\n"
        "s_addc_u32 s37, s31, 0\n"
        "s_mov_b32 s38, 0x110000\n"
        "s_mov_b32 s39, 0x20000\n"
        "s_xor_b32 s51, s51, 8192\n"
        "s_add_u32 s40, s30, s51\n"
        "s_addc_u32 s41, s31, 0\n"
        "s_mov_b32 s42, 0x110000\n"
        "s_mov_b32 s43, 0x20000\n"
        "ds_read_b64_tr_b16 v[64:65], v10 offset:0\n"
        "ds_read_b64_tr_b16 v[66:67], v10 offset:1024\n"
        "ds_read_b64_tr_b16 v[68:69], v10 offset:8192\n"
        "ds_read_b64_tr_b16 v[70:71], v10 offset:9216\n"
        "ds_read_b64_tr_b16 v[72:73], v10 offset:16384\n"
        "ds_read_b64_tr_b16 v[74:75], v10 offset:17408\n"
        "ds_read_b64_tr_b16 v[76:77], v10 offset:24576\n"
        "ds_read_b64_tr_b16 v[78:79], v10 offset:25600\n"
        "ds_read_b64_tr_b16 v[80:81], v10 offset:32768\n"
        "ds_read_b64_tr_b16 v[82:83], v10 offset:33792\n"
        "ds_read_b64_tr_b16 v[84:85], v10 offset:40960\n"
        "ds_read_b64_tr_b16 v[86:87], v10 offset:41984\n"
        "s_waitcnt lgkmcnt(8)\n"
        "ds_read_b64_tr_b16 v[88:89], v10 offset:49152\n"
        "ds_read_b64_tr_b16 v[90:91], v10 offset:50176\n"
        "ds_read_b64_tr_b16 v[92:93], v10 offset:57344\n"
        "ds_read_b64_tr_b16 v[94:95], v10 offset:58368\n"
        "s_waitcnt lgkmcnt(8)\n"
        "ds_read_b64_tr_b16 v[96:97], v11 offset:0\n"
        "ds_read_b64_tr_b16 v[98:99], v11 offset:1024\n"
        "ds_read_b64_tr_b16 v[100:101], v11 offset:8192\n"
        "ds_read_b64_tr_b16 v[102:103], v11 offset:9216\n"
        "s_waitcnt lgkmcnt(8)\n"
        "ds_read_b64_tr_b16 v[104:105], v11 offset:16384\n"
        "ds_read_b64_tr_b16 v[106:107], v11 offset:17408\n"
        "ds_read_b64_tr_b16 v[108:109], v11 offset:24576\n"
        "ds_read_b64_tr_b16 v[110:111], v11 offset:25600\n"
        "s_waitcnt lgkmcnt(8)\n"
        "ds_read_b64_tr_b16 v[112:113], v11 offset:32768\n"
        "ds_read_b64_tr_b16 v[114:115], v11 offset:33792\n"
        "ds_read_b64_tr_b16 v[116:117], v11 offset:40960\n"
        "ds_read_b64_tr_b16 v[118:119], v11 offset:41984\n"
        "s_waitcnt lgkmcnt(8)\n"
        "ds_read_b64_tr_b16 v[120:121], v11 offset:49152\n"
        "ds_read_b64_tr_b16 v[122:123], v11 offset:50176\n"
        "ds_read_b64_tr_b16 v[124:125], v11 offset:57344\n"
        "ds_read_b64_tr_b16 v[126:127], v11 offset:58368\n"
        "s_waitcnt lgkmcnt(8)\n"
        "s_cmp_lg_u32 s7, 0\n"
        "s_cbranch_scc1 .Lfb_DRYRET\n"
        "s_sleep 8\n"
        "s_mov_b32 s58, 65536\n"
        "buffer_load_dwordx4 v[128:131], v28, s[36:39], 0 offen sc1\n"
        "buffer_load_dwordx4 v[132:135], v28, s[36:39], s68 offen sc1\n"
        "buffer_load_dwordx4 v[136:139], v28, s[36:39], s69 offen sc1\n"
        "buffer_load_dwordx4 v[140:143], v28, s[36:39], s70 offen sc1\n"
        "s_waitcnt vmcnt(0)\n"
        "s_branch .Lfb_Q_H1_c\n"
        ".Lfb_Q_H1_r:\n"
        "s_mov_b64 exec, vcc\n"
        "buffer_load_dwordx4 v[128:131], v28, s[36:39], 0 offen sc1\n"
        "buffer_load_dwordx4 v[132:135], v28, s[36:39], s68 offen sc1\n"
        "buffer_load_dwordx4 v[136:139], v28, s[36:39], s69 offen sc1\n"
        "buffer_load_dwordx4 v[140:143], v28, s[36:39], s70 offen sc1\n"
        "s_mov_b64 exec, -1\n"
        "s_sub_u32 s58, s58, 1\n"
        "s_waitcnt vmcnt(0)\n"
        "s_cbranch_scc1 .Lfb_Q_H1_d\n"
        ".Lfb_Q_H1_c:\n"
        "v_add_f32 v144, v128, v132\n"
        "v_add_f32 v145, v130, v134\n"
        "v_add_f32 v146, v136, v140\n"
        "v_add_f32 v147, v138, v142\n"
        "v_xor_b32 v7, s56, v129\n"
        "v_bitop3_b32 v7, v131, s57, v7 bitop3:0xbe\n"
        "v_bitop3_b32 v7, v133, s56, v7 bitop3:0xbe\n"
        "v_bitop3_b32 v7, v135, s57, v7 bitop3:0xbe\n"
        "v_add_f32 v144, v144, v146\n"
        "v_add_f32 v145, v145, v147\n"
        "v_bitop3_b32 v7, v137, s56, v7 bitop3:0xbe\n"
        "v_bitop3_b32 v7, v139, s57, v7 bitop3:0xbe\n"
        "v_bitop3_b32 v7, v141, s56, v7 bitop3:0xbe\n"
        "v_bitop3_b32 v7, v143, s57, v7 bitop3:0xbe\n"
        "v_cvt_pk_bf16_f32 v148, v144, v145\n"
        "v_cmp_ne_u32 vcc, 0, v7\n"
        "s_cbranch_vccnz .Lfb_Q_H1_r\n"
        ".Lfb_Q_H1_d:\n"
        "v_mov_b32 v149, v148\n"
        "s_nop 1\n"
        "v_permlane32_swap_b32 v148, v149\n"
        "v_lshlrev_b32 v144, 16, v149\n"
        "v_and_b32 v145, 0xffff0000, v149\n"
        "v_pk_add_f32 v[144:145], v[144:145], 1.0 op_sel_hi:[1,0]\n"
        "v_lshlrev_b32 v146, 16, v148\n"
        "v_and_b32 v147, 0xffff0000, v148\n"
        "v_cvt_pk_bf16_f32 v149, v144, v145\n"
        "v_lshlrev_b32 v144, 16, v149\n"
        "v_and_b32 v145, 0xffff0000, v149\n"
        "v_pk_mul_f32 v[144:145], v[144:145], v[30:31]\n"
        "v_cvt_pk_bf16_f32 v149, v144, v145\n"
        "v_lshlrev_b32 v144, 16, v149\n"
        "v_and_b32 v145, 0xffff0000, v149\n"
        "v_pk_add_f32 v[144:145], v[144:145], v[146:147]\n"
        "v_cvt_pk_bf16_f32 v149, v144, v145\n"
        "ds_write_b32 v29, v149\n"
        "s_cmp_lg_u32 s7, 0\n"
        "s_cbranch_scc1 .Lfb_DRYRET\n"
        "s_waitcnt lgkmcnt(0)\n"
        "s_barrier\n"
        ".Lfb_QC_S2:\n"
        "ds_read_b128 v[160:163], v16 offset:0\n"
        "ds_read_b128 v[164:167], v16 offset:64\n"
        "ds_read_b128 v[168:171], v16 offset:128\n"
        "ds_read_b128 v[172:175], v16 offset:192\n"
        "ds_read_b128 v[176:179], v16 offset:256\n"
        "ds_read_b128 v[180:183], v16 offset:320\n"
        "ds_read_b128 v[184:187], v16 offset:384\n"
        "ds_read_b128 v[188:191], v16 offset:448\n"
        "s_waitcnt lgkmcnt(7)\n"
        "v_mfma_f32_16x16x32_bf16 v[48:51], v[64:67], v[160:163], 0\n"
        "s_waitcnt lgkmcnt(6)\n"
        "v_mfma_f32_16x16x32_bf16 v[48:51], v[68:71], v[164:167], v[48:51]\n"
        "s_waitcnt lgkmcnt(5)\n"
        "v_mfma_f32_16x16x32_bf16 v[48:51], v[72:75], v[168:171], v[48:51]\n"
        "s_waitcnt lgkmcnt(4)\n"
        "v_mfma_f32_16x16x32_bf16 v[48:51], v[76:79], v[172:175], v[48:51]\n"
        "s_waitcnt lgkmcnt(3)\n"
        "v_mfma_f32_16x16x32_bf16 v[48:51], v[80:83], v[176:179], v[48:51]\n"
        "s_waitcnt lgkmcnt(2)\n"
        "v_mfma_f32_16x16x32_bf16 v[48:51], v[84:87], v[180:183], v[48:51]\n"
        "s_waitcnt lgkmcnt(1)\n"
        "v_mfma_f32_16x16x32_bf16 v[48:51], v[88:91], v[184:187], v[48:51]\n"
        "s_waitcnt lgkmcnt(0)\n"
        "v_mfma_f32_16x16x32_bf16 v[48:51], v[92:95], v[188:191], v[48:51]\n"
        "v_mfma_f32_16x16x32_bf16 v[52:55], v[96:99], v[160:163], 0\n"
        "v_mfma_f32_16x16x32_bf16 v[52:55], v[100:103], v[164:167], v[52:55]\n"
        "v_mfma_f32_16x16x32_bf16 v[52:55], v[104:107], v[168:171], v[52:55]\n"
        "v_mfma_f32_16x16x32_bf16 v[52:55], v[108:111], v[172:175], v[52:55]\n"
        "v_mfma_f32_16x16x32_bf16 v[52:55], v[112:115], v[176:179], v[52:55]\n"
        "v_mfma_f32_16x16x32_bf16 v[52:55], v[116:119], v[180:183], v[52:55]\n"
        "v_mfma_f32_16x16x32_bf16 v[52:55], v[120:123], v[184:187], v[52:55]\n"
        "v_mfma_f32_16x16x32_bf16 v[52:55], v[124:127], v[188:191], v[52:55]\n"
        "v_cndmask_b32 v56, v48, v50, s[60:61]\n"
        "v_cndmask_b32 v58, v49, v51, s[60:61]\n"
        "s_and_b64 exec, s[66:67], s[76:77]\n"
        "buffer_store_dwordx4 v[56:59], v17, s[36:39], 0 offen offset:0 sc1\n"
        "s_mov_b64 exec, s[76:77]\n"
        "s_nop 2\n"
        "v_cndmask_b32 v60, v52, v54, s[60:61]\n"
        "v_cndmask_b32 v62, v53, v55, s[60:61]\n"
        "s_and_b64 exec, s[66:67], s[76:77]\n"
        "buffer_store_dwordx4 v[60:63], v17, s[36:39], 0 offen offset:128 sc1\n"
        "buffer_store_dwordx4 v[56:59], v17, s[40:43], 0 offen offset:0 sc1\n"
        "buffer_store_dwordx4 v[60:63], v17, s[40:43], 0 offen offset:128 sc1\n"
        "s_mov_b64 exec, s[76:77]\n"
        "s_cmp_lg_u32 s7, 0\n"
        "s_cbranch_scc1 .Lfb_DRYRET\n"
        "s_barrier\n"
        "s_cmp_lg_u32 s3, 0\n"
        "s_cbranch_scc1 .Lfb_QC_4\n"
        "v_lshlrev_b32 v10, 7, v1\n"
        "v_add_u32 v10, 8192, v10\n"
        "s_mov_b32 s58, 65536\n"
        "buffer_load_dwordx4 v[12:15], v10, s[32:35], 0 offen sc1\n"
        "s_waitcnt vmcnt(0)\n"
        "s_branch .Lfb_Q_FL_c\n"
        ".Lfb_Q_FL_r:\n"
        "s_mov_b64 exec, vcc\n"
        "s_sleep 2\n"
        "buffer_load_dwordx4 v[12:15], v10, s[32:35], 0 offen sc1\n"
        "s_mov_b64 exec, -1\n"
        "s_sub_u32 s58, s58, 1\n"
        "s_waitcnt vmcnt(0)\n"
        "s_cbranch_scc1 .Lfb_Q_FL_d\n"
        ".Lfb_Q_FL_c:\n"
        "v_xor_b32 v7, s56, v13\n"
        "v_bitop3_b32 v7, v15, s57, v7 bitop3:0xbe\n"
        "v_cmp_ne_u32 vcc, 0, v7\n"
        "s_cbranch_vccnz .Lfb_Q_FL_r\n"
        ".Lfb_Q_FL_d:\n"
        ".Lfb_QC_4:\n"
        "s_barrier\n"
        "ds_read_b64_tr_b16 v[64:65], v20 offset:0\n"
        "ds_read_b64_tr_b16 v[66:67], v20 offset:1024\n"
        "ds_read_b128 v[32:35], v22 offset:0\n"
        "ds_read_b64_tr_b16 v[68:69], v20 offset:8192\n"
        "ds_read_b64_tr_b16 v[70:71], v20 offset:9216\n"
        "ds_read_b128 v[36:39], v22 offset:64\n"
        "ds_read_b64_tr_b16 v[72:73], v20 offset:16384\n"
        "ds_read_b64_tr_b16 v[74:75], v20 offset:17408\n"
        "ds_read_b128 v[40:43], v22 offset:128\n"
        "ds_read_b64_tr_b16 v[76:77], v20 offset:24576\n"
        "ds_read_b64_tr_b16 v[78:79], v20 offset:25600\n"
        "ds_read_b128 v[44:47], v22 offset:192\n"
        "s_waitcnt lgkmcnt(9)\n"
        "v_mfma_f32_16x16x32_bf16 v[48:51], v[64:67], v[32:35], 0\n"
        "ds_read_b64_tr_b16 v[80:81], v21 offset:0\n"
        "ds_read_b64_tr_b16 v[82:83], v21 offset:1024\n"
        "s_waitcnt lgkmcnt(8)\n"
        "v_mfma_f32_16x16x32_bf16 v[48:51], v[68:71], v[36:39], v[48:51]\n"
        "ds_read_b64_tr_b16 v[84:85], v21 offset:8192\n"
        "ds_read_b64_tr_b16 v[86:87], v21 offset:9216\n"
        "s_waitcnt lgkmcnt(7)\n"
        "v_mfma_f32_16x16x32_bf16 v[48:51], v[72:75], v[40:43], v[48:51]\n"
        "ds_read_b64_tr_b16 v[88:89], v21 offset:16384\n"
        "ds_read_b64_tr_b16 v[90:91], v21 offset:17408\n"
        "s_waitcnt lgkmcnt(6)\n"
        "v_mfma_f32_16x16x32_bf16 v[48:51], v[76:79], v[44:47], v[48:51]\n"
        "ds_read_b64_tr_b16 v[92:93], v21 offset:24576\n"
        "ds_read_b64_tr_b16 v[94:95], v21 offset:25600\n"
        "s_waitcnt lgkmcnt(6)\n"
        "v_mfma_f32_16x16x32_bf16 v[52:55], v[80:83], v[32:35], 0\n"
        "s_waitcnt lgkmcnt(4)\n"
        "v_mfma_f32_16x16x32_bf16 v[52:55], v[84:87], v[36:39], v[52:55]\n"
        "s_waitcnt lgkmcnt(2)\n"
        "v_mfma_f32_16x16x32_bf16 v[52:55], v[88:91], v[40:43], v[52:55]\n"
        "s_waitcnt lgkmcnt(0)\n"
        "v_mfma_f32_16x16x32_bf16 v[52:55], v[92:95], v[44:47], v[52:55]\n"
        "s_nop 7\n"
        "v_cndmask_b32 v48, v48, v52, s[60:61]\n"
        "v_cndmask_b32 v49, v49, v53, s[60:61]\n"
        "v_cndmask_b32 v50, v50, v54, s[60:61]\n"
        "v_cndmask_b32 v51, v51, v55, s[60:61]\n"
        "s_and_b64 exec, s[66:67], s[76:77]\n"
        "buffer_store_dwordx4 v[48:51], v23, s[32:35], 0 offen offset:0 sc1\n"
        "s_endpgm\n"
        ".Lfb_ROLE_F:\n"
        "s_sub_u32 s49, s49, 24\n"
        "s_cmp_lt_u32 s3, 4\n"
        "s_cbranch_scc1 .Lfb_FM_C\n"
        "s_and_b32 s52, s50, 1\n"
        "s_lshl_b32 s52, s52, 2\n"
        "v_lshrrev_b32 v4, 3, v1\n"
        "v_and_b32 v5, 2, v4\n"
        "v_or_b32 v5, s52, v5\n"
        "v_and_b32 v6, 7, v1\n"
        "v_xor_b32 v5, v6, v5\n"
        "v_mul_u32_u24 v4, 6144, v4\n"
        "v_lshl_add_u32 v10, v5, 4, v4\n"
        "s_and_b32 s52, s49, 3\n"
        "s_mul_i32 s52, s52, 786432\n"
        "s_lshl_b32 s53, s48, 8\n"
        "s_add_u32 s52, s52, s53\n"
        "s_lshr_b32 s53, s49, 2\n"
        "s_lshl_b32 s53, s53, 7\n"
        "s_add_u32 s52, s52, s53\n"
        "s_lshl_b32 s53, s50, 10\n"
        "s_mov_b32 m0, s53\n"
        "s_mul_i32 s59, s50, 49152\n"
        "s_mov_b32 s68, 196608\n"
        "s_waitcnt lgkmcnt(0)\n"
        "s_add_u32 s36, s26, s52\n"
        "s_addc_u32 s37, s27, 0\n"
        "s_mov_b32 s38, 0xc0000\n"
        "s_mov_b32 s39, 0x20000\n"
        "buffer_load_dwordx4 v10, s[36:39], s59 offen nt lds\n"
        "s_add_u32 m0, m0, 4096\n"
        "s_add_u32 s59, s59, s68\n"
        "buffer_load_dwordx4 v10, s[36:39], s59 offen nt lds\n"
        "s_add_u32 m0, m0, 4096\n"
        "s_add_u32 s59, s59, s68\n"
        "buffer_load_dwordx4 v10, s[36:39], s59 offen nt lds\n"
        "s_add_u32 m0, m0, 4096\n"
        "s_add_u32 s59, s59, s68\n"
        "buffer_load_dwordx4 v10, s[36:39], s59 offen nt lds\n"
        "s_add_u32 m0, m0, 4096\n"
        "s_add_u32 s59, s59, s68\n"
        "s_mov_b32 s32, s30\n"
        "s_mov_b32 s33, s31\n"
        "s_lshl_b32 s52, s48, 3\n"
        "s_add_u32 s52, s52, s49\n"
        "s_lshl_b32 s53, s52, 14\n"
        "s_lshl_b32 s54, s50, 12\n"
        "s_add_u32 s53, s53, s54\n"
        "s_add_u32 s53, s53, 229376\n"
        "v_lshl_add_u32 v10, v1, 4, s53\n"
        "v_mov_b32 v16, -1\n"
        "v_mov_b32 v17, -1\n"
        "v_mov_b32 v18, -1\n"
        "v_mov_b32 v19, -1\n"
        "buffer_store_dwordx4 v[16:19], v10, s[32:35], 0 offen offset:0 sc1\n"
        "buffer_store_dwordx4 v[16:19], v10, s[32:35], 0 offen offset:1024 sc1\n"
        "buffer_store_dwordx4 v[16:19], v10, s[32:35], 0 offen offset:2048 sc1\n"
        "buffer_store_dwordx4 v[16:19], v10, s[32:35], 0 offen offset:3072 sc1\n"
        "s_mov_b32 s5, s52\n"
        "s_waitcnt lgkmcnt(0)\n"
        "s_mov_b32 s7, 1\n"
        "s_mov_b64 s[76:77], 0\n"
        "s_mov_b64 exec, 0\n"
        "s_mov_b32 s34, 0\n"
        "s_mov_b32 s36, s30\n"
        "s_mov_b32 s37, s31\n"
        "s_mov_b32 s38, 0\n"
        "s_mov_b32 s39, 0x20000\n"
        "s_mov_b32 s40, s30\n"
        "s_mov_b32 s41, s31\n"
        "s_mov_b32 s42, 0\n"
        "s_mov_b32 s43, 0x20000\n"
        "s_cmp_lg_u32 s50, 0\n"
        "s_cbranch_scc1 .Lfb_FL_D_0\n"
        "s_call_b64 s[26:27], .Lfb_FM_S1\n"
        "s_branch .Lfb_FL_D_x\n"
        ".Lfb_FL_D_0:\n"
        "s_cmp_lg_u32 s50, 1\n"
        "s_cbranch_scc1 .Lfb_FL_D_1\n"
        "s_call_b64 s[26:27], .Lfb_FM_B\n"
        "s_branch .Lfb_FL_D_x\n"
        ".Lfb_FL_D_1:\n"
        "s_cmp_lg_u32 s50, 2\n"
        "s_cbranch_scc1 .Lfb_FL_D_2\n"
        "s_branch .Lfb_FL_D_x\n"
        ".Lfb_FL_D_2:\n"
        "s_cmp_lg_u32 s50, 3\n"
        "s_cbranch_scc1 .Lfb_FL_D_3\n"
        "s_branch .Lfb_FL_D_x\n"
        ".Lfb_FL_D_3:\n"
        ".Lfb_FL_D_x:\n"
        "s_waitcnt vmcnt(63) lgkmcnt(0)\n"
        "s_mov_b32 s7, 0\n"
        "s_mov_b32 s6, 0\n"
        "s_mov_b64 s[76:77], -1\n"
        "s_mov_b64 exec, -1\n"
        "s_mov_b32 s34, 1114112\n"
        "s_mov_b32 s52, s5\n"
        "s_waitcnt vmcnt(4)\n"
        "s_barrier\n"
        "v_mov_b32 v8, 135552\n"
        "ds_read_b64 v[8:9], v8\n"
        "s_waitcnt lgkmcnt(0)\n"
        "v_readfirstlane_b32 s56, v8\n"
        "v_readfirstlane_b32 s57, v9\n"
        "s_waitcnt vmcnt(0)\n"
        "s_barrier\n"
        "s_cmp_lg_u32 s3, 4\n"
        "s_cbranch_scc1 .Lfb_F_2\n"
        "s_lshl_b32 s52, s52, 7\n"
        "s_add_u32 s52, s52, 8192\n"
        "v_mov_b32 v14, v8\n"
        "v_mov_b32 v15, v8\n"
        "v_mov_b32 v16, v9\n"
        "v_mov_b32 v17, v9\n"
        "s_mov_b64 exec, 1\n"
        "buffer_store_dwordx4 v[14:17], off, s[32:35], s52 sc1\n"
        "s_mov_b64 exec, -1\n"
        "s_branch .Lfb_F_2\n"
        ".Lfb_FM_C:\n"
        "s_cmp_gt_u32 s3, 1\n"
        "s_cbranch_scc1 .Lfb_FM_1\n"
        "s_cmp_eq_u32 s3, 1\n"
        "s_cbranch_scc1 .Lfb_FM_S\n"
        "s_waitcnt lgkmcnt(0)\n"
        "s_mov_b32 s32, s30\n"
        "s_mov_b32 s33, s31\n"
        "buffer_load_dwordx2 v[12:13], off, s[32:35], 0 sc1\n"
        "s_waitcnt vmcnt(0)\n"
        "v_readfirstlane_b32 s56, v12\n"
        "v_readfirstlane_b32 s57, v13\n"
        "s_add_u32 s56, s56, 1\n"
        "s_addc_u32 s57, s57, 0\n"
        "v_mov_b32 v12, s56\n"
        "v_mov_b32 v13, s57\n"
        "v_mov_b32 v9, 135552\n"
        "ds_write_b64 v9, v[12:13]\n"
        "s_branch .Lfb_FM_1\n"
        ".Lfb_FM_S:\n"
        "s_and_b32 s52, s49, 3\n"
        "s_lshl_b32 s52, s52, 8\n"
        "v_lshl_add_u32 v5, v1, 2, s52\n"
        "s_waitcnt lgkmcnt(0)\n"
        "global_load_dword v12, v5, s[24:25]\n"
        "s_waitcnt vmcnt(0)\n"
        "v_lshlrev_b32 v13, 16, v12\n"
        "v_and_b32 v14, 0xffff0000, v12\n"
        "v_mul_f32 v15, 0xbfb8aa3b, v13\n"
        "v_mul_f32 v16, 0xbfb8aa3b, v14\n"
        "v_exp_f32 v15, v15\n"
        "v_exp_f32 v16, v16\n"
        "v_add_f32 v15, 1.0, v15\n"
        "v_add_f32 v16, 1.0, v16\n"
        "v_rcp_f32 v15, v15\n"
        "v_rcp_f32 v16, v16\n"
        "v_mul_f32 v13, v13, v15\n"
        "v_mul_f32 v14, v14, v16\n"
        "v_cvt_pk_bf16_f32 v13, v13, v14\n"
        "v_lshlrev_b32 v14, 2, v1\n"
        "v_add_u32 v14, 134336, v14\n"
        "ds_write_b32 v14, v13\n"
        ".Lfb_FM_1:\n"
        "s_waitcnt lgkmcnt(0)\n"
        "s_mov_b32 s32, s30\n"
        "s_mov_b32 s33, s31\n"
        ".Lfb_FM_B:\n"
        "s_mov_b32 s51, 0\n"
        "s_add_u32 s36, s30, s51\n"
        "s_addc_u32 s37, s31, 0\n"
        "s_mov_b32 s38, 0x110000\n"
        "s_mov_b32 s39, 0x20000\n"
        "s_xor_b32 s51, s51, 8192\n"
        "s_add_u32 s40, s30, s51\n"
        "s_addc_u32 s41, s31, 0\n"
        "s_mov_b32 s42, 0x110000\n"
        "s_mov_b32 s43, 0x20000\n"
        "s_lshl_b32 s52, s50, 1\n"
        "v_lshrrev_b32 v4, 2, v3\n"
        "v_lshl_add_u32 v5, v2, 3, v4\n"
        "v_lshlrev_b32 v5, 7, v5\n"
        "v_and_b32 v4, 2, v4\n"
        "v_and_b32 v6, 1, v2\n"
        "v_lshl_or_b32 v4, v6, 2, v4\n"
        "v_bfe_u32 v6, v1, 1, 1\n"
        "v_or_b32 v6, s52, v6\n"
        "v_xor_b32 v4, v6, v4\n"
        "v_lshl_add_u32 v5, v4, 4, v5\n"
        "v_and_b32 v6, 1, v1\n"
        "v_lshl_add_u32 v20, v6, 3, v5\n"
        "v_lshlrev_b32 v4, 4, v2\n"
        "v_add_u32 v22, 134336, v4\n"
        "s_mov_b32 s60, 0xaaaaaaaa\n"
        "s_mov_b32 s61, 0xaaaaaaaa\n"
        "s_mov_b32 s66, 0x30003\n"
        "s_mov_b32 s67, 0x30003\n"
        "s_and_b32 s52, s49, 3\n"
        "s_lshl_b32 s52, s52, 14\n"
        "s_lshl_b32 s53, s48, 10\n"
        "s_add_u32 s52, s52, s53\n"
        "s_lshr_b32 s53, s49, 2\n"
        "s_lshl_b32 s53, s53, 9\n"
        "s_add_u32 s52, s52, s53\n"
        "s_lshl_b32 s53, s50, 7\n"
        "s_add_u32 s52, s52, s53\n"
        "s_add_u32 s52, s52, 32768\n"
        "v_and_b32 v5, 1, v1\n"
        "v_lshl_add_u32 v6, v2, 1, v5\n"
        "v_lshl_add_u32 v17, v6, 4, s52\n"
        "v_mov_b32 v8, 135552\n"
        "s_cmp_lg_u32 s7, 0\n"
        "s_cbranch_scc1 .Lfb_DRYRET\n"
        "s_waitcnt lgkmcnt(0)\n"
        "s_barrier\n"
        ".Lfb_FM_S1:\n"
        "ds_read_b64_tr_b16 v[64:65], v20 offset:0\n"
        "ds_read_b64_tr_b16 v[66:67], v20 offset:512\n"
        "ds_read_b128 v[32:35], v22 offset:0\n"
        "ds_read_b64_tr_b16 v[68:69], v20 offset:4096\n"
        "ds_read_b64_tr_b16 v[70:71], v20 offset:4608\n"
        "ds_read_b128 v[36:39], v22 offset:64\n"
        "ds_read_b64_tr_b16 v[72:73], v20 offset:8192\n"
        "ds_read_b64_tr_b16 v[74:75], v20 offset:8704\n"
        "ds_read_b128 v[40:43], v22 offset:128\n"
        "ds_read_b64_tr_b16 v[76:77], v20 offset:12288\n"
        "ds_read_b64_tr_b16 v[78:79], v20 offset:12800\n"
        "ds_read_b128 v[44:47], v22 offset:192\n"
        "ds_read_b64 v[60:61], v8\n"
        "s_waitcnt lgkmcnt(10)\n"
        "v_mfma_f32_16x16x32_bf16 v[48:51], v[64:67], v[32:35], 0\n"
        "s_waitcnt lgkmcnt(7)\n"
        "v_mfma_f32_16x16x32_bf16 v[48:51], v[68:71], v[36:39], v[48:51]\n"
        "s_waitcnt lgkmcnt(4)\n"
        "v_mfma_f32_16x16x32_bf16 v[48:51], v[72:75], v[40:43], v[48:51]\n"
        "s_waitcnt lgkmcnt(1)\n"
        "v_mfma_f32_16x16x32_bf16 v[48:51], v[76:79], v[44:47], v[48:51]\n"
        "s_waitcnt lgkmcnt(0)\n"
        "v_mov_b32 v57, v60\n"
        "v_mov_b32 v59, v61\n"
        "s_nop 4\n"
        "v_cndmask_b32 v56, v48, v50, s[60:61]\n"
        "v_cndmask_b32 v58, v49, v51, s[60:61]\n"
        "s_and_b64 exec, s[66:67], s[76:77]\n"
        "buffer_store_dwordx4 v[56:59], v17, s[36:39], 0 offen offset:0 sc1\n"
        "buffer_store_dwordx4 v[56:59], v17, s[40:43], 0 offen offset:0 sc1\n"
        "s_mov_b64 exec, s[76:77]\n"
        "s_cmp_lg_u32 s7, 0\n"
        "s_cbranch_scc1 .Lfb_DRYRET\n"
        "s_barrier\n"
        ".Lfb_F_2:\n"
        "s_lshl_b32 s5, s48, 3\n"
        "s_add_u32 s5, s5, s49\n"
        "s_cmp_lt_u32 s48, 4\n"
        "s_cbranch_scc1 .Lfb_F_3\n"
        "s_mul_i32 s5, s48, 6\n"
        "s_add_u32 s5, s5, s49\n"
        "s_add_u32 s5, s5, 8\n"
        "s_cmp_lt_u32 s49, 6\n"
        "s_cbranch_scc1 .Lfb_F_3\n"
        "s_endpgm\n"
        ".Lfb_F_3:\n"
        "v_lshrrev_b32 v4, 2, v3\n"
        "v_lshl_add_u32 v5, v2, 3, v4\n"
        "v_bfe_u32 v6, v1, 1, 1\n"
        "v_and_b32 v7, 1, v1\n"
        "v_lshlrev_b32 v7, 3, v7\n"
        "v_and_b32 v28, 1, v2\n"
        "s_cmp_lt_u32 s3, 4\n"
        "s_cbranch_scc1 .Lfb_F_5\n"
        "v_lshrrev_b32 v10, 2, v1\n"
        "v_mul_u32_u24 v10, 3584, v10\n"
        "v_and_b32 v11, 3, v1\n"
        "v_lshrrev_b32 v12, 5, v1\n"
        "v_xor_b32 v11, v11, v12\n"
        "v_lshl_add_u32 v26, v11, 4, v10\n"
        "v_xor_b32 v11, 2, v11\n"
        "v_lshl_add_u32 v27, v11, 4, v10\n"
        "s_lshl_b32 s52, s5, 6\n"
        "s_add_u32 s36, s18, s52\n"
        "s_addc_u32 s37, s19, 0\n"
        "s_mov_b32 s38, 0x1c0000\n"
        "s_mov_b32 s39, 0x20000\n"
        "s_add_u32 s40, s20, s52\n"
        "s_addc_u32 s41, s21, 0\n"
        "s_mov_b32 s42, 0x1c0000\n"
        "s_mov_b32 s43, 0x20000\n"
        "s_lshl_b32 s52, s50, 10\n"
        "s_mul_i32 s55, s50, 57344\n"
        "s_add_u32 m0, s52, 0\n"
        "s_add_u32 s59, s55, 0\n"
        "buffer_load_dwordx4 v26, s[36:39], s59 offen nt lds\n"
        "s_add_u32 m0, s52, 32768\n"
        "s_nop 0\n"
        "buffer_load_dwordx4 v27, s[40:43], s59 offen nt lds\n"
        "s_add_u32 m0, s52, 4096\n"
        "s_add_u32 s59, s55, 229376\n"
        "buffer_load_dwordx4 v26, s[36:39], s59 offen nt lds\n"
        "s_add_u32 m0, s52, 36864\n"
        "s_nop 0\n"
        "buffer_load_dwordx4 v27, s[40:43], s59 offen nt lds\n"
        "s_add_u32 m0, s52, 8192\n"
        "s_add_u32 s59, s55, 458752\n"
        "buffer_load_dwordx4 v26, s[36:39], s59 offen nt lds\n"
        "s_add_u32 m0, s52, 40960\n"
        "s_nop 0\n"
        "buffer_load_dwordx4 v27, s[40:43], s59 offen nt lds\n"
        "s_add_u32 m0, s52, 12288\n"
        "s_add_u32 s59, s55, 688128\n"
        "buffer_load_dwordx4 v26, s[36:39], s59 offen nt lds\n"
        "s_add_u32 m0, s52, 45056\n"
        "s_nop 0\n"
        "buffer_load_dwordx4 v27, s[40:43], s59 offen nt lds\n"
        "s_lshl_b32 s53, s50, 13\n"
        "s_add_u32 s53, s53, 98304\n"
        "v_lshl_add_u32 v5, v5, 8, v7\n"
        "v_lshl_add_u32 v5, v6, 4, v5\n"
        "v_add_u32 v5, s53, v5\n"
        "v_lshl_or_b32 v4, v28, 2, v4\n"
        "v_xor_b32 v6, 0, v4\n"
        "v_lshl_add_u32 v208, v6, 5, v5\n"
        "v_xor_b32 v6, 1, v4\n"
        "v_lshl_add_u32 v209, v6, 5, v5\n"
        "v_xor_b32 v6, 2, v4\n"
        "v_lshl_add_u32 v210, v6, 5, v5\n"
        "v_xor_b32 v6, 3, v4\n"
        "v_lshl_add_u32 v211, v6, 5, v5\n"
        "v_xor_b32 v6, 4, v4\n"
        "v_lshl_add_u32 v212, v6, 5, v5\n"
        "v_xor_b32 v6, 5, v4\n"
        "v_lshl_add_u32 v213, v6, 5, v5\n"
        "v_xor_b32 v6, 6, v4\n"
        "v_lshl_add_u32 v214, v6, 5, v5\n"
        "v_xor_b32 v6, 7, v4\n"
        "v_lshl_add_u32 v215, v6, 5, v5\n"
        "v_lshlrev_b32 v4, 4, v2\n"
        "v_add_u32 v5, 134272, v4\n"
        "v_add_u32 v6, 134208, v4\n"
        "s_mov_b32 s66, 0x10001\n"
        "s_mov_b32 s67, 0x10001\n"
        "v_cndmask_b32 v216, v5, v6, s[66:67]\n"
        "s_mov_b32 s66, 0x20002\n"
        "s_mov_b32 s67, 0x20002\n"
        "v_cndmask_b32 v217, v5, v6, s[66:67]\n"
        "s_mov_b32 s66, 0x40004\n"
        "s_mov_b32 s67, 0x40004\n"
        "v_cndmask_b32 v218, v5, v6, s[66:67]\n"
        "s_mov_b32 s66, 0x80008\n"
        "s_mov_b32 s67, 0x80008\n"
        "v_cndmask_b32 v219, v5, v6, s[66:67]\n"
        "s_mov_b32 s66, 0x100010\n"
        "s_mov_b32 s67, 0x100010\n"
        "v_cndmask_b32 v220, v5, v6, s[66:67]\n"
        "s_mov_b32 s66, 0x200020\n"
        "s_mov_b32 s67, 0x200020\n"
        "v_cndmask_b32 v221, v5, v6, s[66:67]\n"
        "s_mov_b32 s66, 0x400040\n"
        "s_mov_b32 s67, 0x400040\n"
        "v_cndmask_b32 v222, v5, v6, s[66:67]\n"
        "s_mov_b32 s66, 0x800080\n"
        "s_mov_b32 s67, 0x800080\n"
        "v_cndmask_b32 v223, v5, v6, s[66:67]\n"
        "v_lshlrev_b32 v5, 2, v3\n"
        "v_add_u32 v5, 134272, v5\n"
        "v_mov_b32 v6, 0\n"
        "ds_write_b32 v5, v6\n"
        "v_lshl_add_u32 v4, v3, 6, v4\n"
        "s_lshl_b32 s54, s5, 11\n"
        "s_and_b32 s51, s54, 0xffffe000\n"
        "s_add_u32 s54, s54, s51\n"
        "s_lshl_b32 s51, s50, 9\n"
        "s_add_u32 s54, s54, s51\n"
        "s_add_u32 s54, s54, 884736\n"
        "v_add_u32 v20, s54, v4\n"
        "s_mov_b32 s66, 0xff00ff\n"
        "s_mov_b32 s67, 0xff00ff\n"
        "v_lshlrev_b32 v4, 1, v2\n"
        "v_xor_b32 v4, v3, v4\n"
        "v_lshlrev_b32 v4, 4, v4\n"
        "v_lshl_add_u32 v23, v2, 10, v4\n"
        "v_xor_b32 v24, 128, v23\n"
        "s_lshl_b32 s54, s5, 15\n"
        "s_lshl_b32 s51, s50, 8\n"
        "s_add_u32 s54, s54, s51\n"
        "s_add_u32 s44, s22, s54\n"
        "s_addc_u32 s45, s23, 0\n"
        "s_mov_b32 s46, 0x8000\n"
        "s_mov_b32 s47, 0x20000\n"
        "s_lshr_b32 s54, s5, 1\n"
        "s_and_b32 s54, s54, 3\n"
        "s_cmp_eq_u32 s54, 3\n"
        "s_cbranch_scc1 .Lfb_F_P1\n"
        ".Lfb_F_DEL2:\n"
        "s_memrealtime vcc\n"
        "s_waitcnt lgkmcnt(0)\n"
        "s_sub_u32 vcc_lo, vcc_lo, s4\n"
        "s_cmp_ge_u32 vcc_lo, 460\n"
        "s_cbranch_scc1 .Lfb_F_DEL2_x\n"
        "s_sleep 1\n"
        "s_branch .Lfb_F_DEL2\n"
        ".Lfb_F_DEL2_x:\n"
        ".Lfb_F_P1:\n"
        "s_add_u32 m0, s52, 16384\n"
        "s_add_u32 s59, s55, 917504\n"
        "buffer_load_dwordx4 v26, s[36:39], s59 offen nt lds\n"
        "s_add_u32 m0, s52, 49152\n"
        "s_nop 0\n"
        "buffer_load_dwordx4 v27, s[40:43], s59 offen nt lds\n"
        "s_add_u32 m0, s52, 20480\n"
        "s_add_u32 s59, s55, 1146880\n"
        "buffer_load_dwordx4 v26, s[36:39], s59 offen nt lds\n"
        "s_add_u32 m0, s52, 53248\n"
        "s_nop 0\n"
        "buffer_load_dwordx4 v27, s[40:43], s59 offen nt lds\n"
        ".Lfb_F_DEL4:\n"
        "s_memrealtime vcc\n"
        "s_waitcnt lgkmcnt(0)\n"
        "s_sub_u32 vcc_lo, vcc_lo, s4\n"
        "s_cmp_ge_u32 vcc_lo, 460\n"
        "s_cbranch_scc1 .Lfb_F_DEL4_x\n"
        "s_sleep 1\n"
        "s_branch .Lfb_F_DEL4\n"
        ".Lfb_F_DEL4_x:\n"
        "s_add_u32 m0, s52, 24576\n"
        "s_add_u32 s59, s55, 1376256\n"
        "buffer_load_dwordx4 v26, s[36:39], s59 offen nt lds\n"
        "s_add_u32 m0, s52, 57344\n"
        "s_nop 0\n"
        "buffer_load_dwordx4 v27, s[40:43], s59 offen nt lds\n"
        "s_add_u32 m0, s52, 28672\n"
        "s_add_u32 s59, s55, 1605632\n"
        "buffer_load_dwordx4 v26, s[36:39], s59 offen nt lds\n"
        "s_add_u32 m0, s52, 61440\n"
        "s_nop 0\n"
        "buffer_load_dwordx4 v27, s[40:43], s59 offen nt lds\n"
        "s_mov_b32 m0, s53\n"
        "s_mov_b32 s59, 0\n"
        "buffer_load_dwordx4 v23, s[44:47], s59 offen nt lds\n"
        "s_add_u32 m0, m0, 1024\n"
        "s_add_u32 s59, s59, 4096\n"
        "buffer_load_dwordx4 v23, s[44:47], s59 offen nt lds\n"
        "s_add_u32 m0, m0, 1024\n"
        "s_add_u32 s59, s59, 4096\n"
        "buffer_load_dwordx4 v24, s[44:47], s59 offen nt lds\n"
        "s_add_u32 m0, m0, 1024\n"
        "s_add_u32 s59, s59, 4096\n"
        "buffer_load_dwordx4 v24, s[44:47], s59 offen nt lds\n"
        "s_add_u32 m0, m0, 1024\n"
        "s_add_u32 s59, s59, 4096\n"
        "buffer_load_dwordx4 v23, s[44:47], s59 offen nt lds\n"
        "s_add_u32 m0, m0, 1024\n"
        "s_add_u32 s59, s59, 4096\n"
        "buffer_load_dwordx4 v23, s[44:47], s59 offen nt lds\n"
        "s_add_u32 m0, m0, 1024\n"
        "s_add_u32 s59, s59, 4096\n"
        "buffer_load_dwordx4 v24, s[44:47], s59 offen nt lds\n"
        "s_add_u32 m0, m0, 1024\n"
        "s_add_u32 s59, s59, 4096\n"
        "buffer_load_dwordx4 v24, s[44:47], s59 offen nt lds\n"
        "s_add_u32 m0, m0, 1024\n"
        "s_add_u32 s59, s59, 4096\n"
        "s_barrier\n"
        "s_mov_b32 s51, 0\n"
        "s_add_u32 s36, s30, s51\n"
        "s_addc_u32 s37, s31, 0\n"
        "s_mov_b32 s38, 0x110000\n"
        "s_mov_b32 s39, 0x20000\n"
        "s_xor_b32 s51, s51, 8192\n"
        "s_add_u32 s40, s30, s51\n"
        "s_addc_u32 s41, s31, 0\n"
        "s_mov_b32 s42, 0x110000\n"
        "s_mov_b32 s43, 0x20000\n"
        "s_waitcnt vmcnt(8)\n"
        "s_barrier\n"
        "s_waitcnt vmcnt(0)\n"
        "ds_read_b64_tr_b16 v[64:65], v208 offset:0\n"
        "ds_read_b64_tr_b16 v[66:67], v208 offset:1024\n"
        "ds_read_b64_tr_b16 v[68:69], v209 offset:0\n"
        "ds_read_b64_tr_b16 v[70:71], v209 offset:1024\n"
        "ds_read_b64_tr_b16 v[72:73], v210 offset:0\n"
        "ds_read_b64_tr_b16 v[74:75], v210 offset:1024\n"
        "ds_read_b64_tr_b16 v[76:77], v211 offset:0\n"
        "ds_read_b64_tr_b16 v[78:79], v211 offset:1024\n"
        "ds_read_b64_tr_b16 v[80:81], v212 offset:0\n"
        "ds_read_b64_tr_b16 v[82:83], v212 offset:1024\n"
        "ds_read_b64_tr_b16 v[84:85], v213 offset:0\n"
        "ds_read_b64_tr_b16 v[86:87], v213 offset:1024\n"
        "s_waitcnt lgkmcnt(4)\n"
        "ds_read_b64_tr_b16 v[88:89], v214 offset:0\n"
        "ds_read_b64_tr_b16 v[90:91], v214 offset:1024\n"
        "ds_read_b64_tr_b16 v[92:93], v215 offset:0\n"
        "ds_read_b64_tr_b16 v[94:95], v215 offset:1024\n"
        "s_waitcnt lgkmcnt(0)\n"
        "s_barrier\n"
        "s_barrier\n"
        "s_barrier\n"
        ".Lfb_FL_S:\n"
        "ds_read_b128 v[144:147], v216\n"
        "ds_read_b128 v[148:151], v217\n"
        "ds_read_b128 v[152:155], v218\n"
        "ds_read_b128 v[156:159], v219\n"
        "ds_read_b128 v[160:163], v220\n"
        "ds_read_b128 v[164:167], v221\n"
        "ds_read_b128 v[168:171], v222\n"
        "ds_read_b128 v[172:175], v223\n"
        "s_waitcnt lgkmcnt(7)\n"
        "v_mfma_f32_16x16x32_bf16 v[48:51], v[64:67], v[144:147], 0\n"
        "s_waitcnt lgkmcnt(6)\n"
        "v_mfma_f32_16x16x32_bf16 v[48:51], v[68:71], v[148:151], v[48:51]\n"
        "s_waitcnt lgkmcnt(5)\n"
        "v_mfma_f32_16x16x32_bf16 v[48:51], v[72:75], v[152:155], v[48:51]\n"
        "s_waitcnt lgkmcnt(4)\n"
        "v_mfma_f32_16x16x32_bf16 v[48:51], v[76:79], v[156:159], v[48:51]\n"
        "s_waitcnt lgkmcnt(3)\n"
        "v_mfma_f32_16x16x32_bf16 v[48:51], v[80:83], v[160:163], v[48:51]\n"
        "s_waitcnt lgkmcnt(2)\n"
        "v_mfma_f32_16x16x32_bf16 v[48:51], v[84:87], v[164:167], v[48:51]\n"
        "s_waitcnt lgkmcnt(1)\n"
        "v_mfma_f32_16x16x32_bf16 v[48:51], v[88:91], v[168:171], v[48:51]\n"
        "s_waitcnt lgkmcnt(0)\n"
        "v_mfma_f32_16x16x32_bf16 v[48:51], v[92:95], v[172:175], v[48:51]\n"
        "s_and_b64 exec, s[66:67], s[76:77]\n"
        "s_nop 6\n"
        "buffer_store_dwordx4 v[48:51], v20, s[36:39], 0 offen offset:0 sc1\n"
        "buffer_store_dwordx4 v[48:51], v20, s[40:43], 0 offen offset:0 sc1\n"
        "s_cmp_lg_u32 s7, 0\n"
        "s_cbranch_scc1 .Lfb_DRYRET\n"
        "s_endpgm\n"
        ".Lfb_F_5:\n"
        "v_lshl_add_u32 v24, v5, 6, v7\n"
        "v_lshl_add_u32 v24, v6, 15, v24\n"
        "v_lshl_or_b32 v6, v6, 1, v28\n"
        "v_xor_b32 v6, s50, v6\n"
        "v_lshl_add_u32 v24, v6, 4, v24\n"
        "s_lshl_b32 s52, s50, 4\n"
        "s_add_u32 s52, s52, 134208\n"
        "v_lshrrev_b32 v5, 5, v1\n"
        "v_lshl_add_u32 v4, v28, 1, v5\n"
        "v_lshl_add_u32 v23, v4, 2, s52\n"
        "s_lshl_b32 s52, s50, 3\n"
        "s_add_u32 s52, s52, 134144\n"
        "v_lshl_add_u32 v21, v5, 2, s52\n"
        "v_mov_b32 v22, 134144\n"
        "v_lshlrev_b32 v4, 4, v2\n"
        "v_add_u32 v18, 131072, v4\n"
        "s_lshl_b32 s52, s50, 7\n"
        "v_lshl_add_u32 v4, v3, 2, v2\n"
        "v_add_u32 v9, s52, v4\n"
        "v_lshlrev_b32 v30, 1, v9\n"
        "v_lshlrev_b32 v26, 2, v9\n"
        "v_add_u32 v26, 132096, v26\n"
        "v_add_u32 v11, 131072, v30\n"
        "s_lshl_b32 s52, s50, 9\n"
        "v_lshlrev_b32 v4, 4, v3\n"
        "v_add_u32 v4, s52, v4\n"
        "v_lshl_add_u32 v27, v2, 13, v4\n"
        "v_add_u32 v27, 229376, v27\n"
        "v_lshl_add_u32 v19, v2, 11, v4\n"
        "v_add_u32 v19, 819200, v19\n"
        "s_and_b32 s52, s5, 15\n"
        "s_lshl_b32 s52, s52, 7\n"
        "v_and_b32 v5, 7, v1\n"
        "v_lshl_add_u32 v6, v5, 4, s52\n"
        "v_lshl_add_u32 v20, v2, 13, v6\n"
        "v_add_u32 v20, 235520, v20\n"
        "v_lshlrev_b32 v29, 7, v5\n"
        "v_add_u32 v29, 16384, v29\n"
        "v_lshrrev_b32 v4, 3, v1\n"
        "v_lshl_add_u32 v25, v4, 11, v6\n"
        "v_lshrrev_b32 v4, 2, v4\n"
        "v_lshl_add_u32 v25, v4, 13, v25\n"
        "v_add_u32 v25, 884736, v25\n"
        "v_lshl_add_u32 v6, v2, 2, v6\n"
        "v_add_u32 v16, 132096, v6\n"
        "v_lshrrev_b32 v17, 1, v6\n"
        "s_and_b32 s52, s5, 3\n"
        "s_add_u32 s68, s52, 0\n"
        "s_and_b32 s68, s68, 3\n"
        "s_lshl_b32 s68, s68, 14\n"
        "s_add_u32 s69, s52, 1\n"
        "s_and_b32 s69, s69, 3\n"
        "s_lshl_b32 s69, s69, 14\n"
        "s_add_u32 s70, s52, 2\n"
        "s_and_b32 s70, s70, 3\n"
        "s_lshl_b32 s70, s70, 14\n"
        "s_add_u32 s71, s52, 3\n"
        "s_and_b32 s71, s71, 3\n"
        "s_lshl_b32 s71, s71, 14\n"
        "s_mov_b32 s73, 4096\n"
        "s_mov_b32 s72, 0x3b000000\n"
        "s_mov_b32 s74, 0xbdd2d3e8\n"
        "v_mov_b32 v62, 0xc0135761\n"
        "v_mov_b32 v63, 0xc0135761\n"
        "s_mov_b32 s6, 0\n"
        "s_mov_b64 s[62:63], s[72:73]\n"
        "s_mov_b64 s[64:65], s[68:69]\n"
        "s_mov_b64 s[66:67], s[70:71]\n"
        "s_waitcnt lgkmcnt(0)\n"
        "s_mov_b32 s7, 1\n"
        "s_mov_b64 s[76:77], 0\n"
        "s_mov_b64 exec, 0\n"
        "s_mov_b32 s34, 0\n"
        "s_mov_b32 s36, s30\n"
        "s_mov_b32 s37, s31\n"
        "s_mov_b32 s38, 0\n"
        "s_mov_b32 s39, 0x20000\n"
        "s_mov_b32 s40, s30\n"
        "s_mov_b32 s41, s31\n"
        "s_mov_b32 s42, 0\n"
        "s_mov_b32 s43, 0x20000\n"
        "s_cmp_lg_u32 s50, 0\n"
        "s_cbranch_scc1 .Lfb_FC_D_0\n"
        "s_call_b64 s[26:27], .Lfb_FC_S0\n"
        "s_call_b64 s[26:27], .Lfb_FL_S\n"
        "s_branch .Lfb_FC_D_x\n"
        ".Lfb_FC_D_0:\n"
        "s_cmp_lg_u32 s50, 1\n"
        "s_cbranch_scc1 .Lfb_FC_D_1\n"
        "s_call_b64 s[26:27], .Lfb_FC_S1\n"
        "s_call_b64 s[26:27], .Lfb_F_R\n"
        "s_branch .Lfb_FC_D_x\n"
        ".Lfb_FC_D_1:\n"
        "s_cmp_lg_u32 s50, 2\n"
        "s_cbranch_scc1 .Lfb_FC_D_2\n"
        "s_call_b64 s[26:27], .Lfb_FC_S2\n"
        "s_call_b64 s[26:27], .Lfb_FC_S3\n"
        "s_branch .Lfb_FC_D_x\n"
        ".Lfb_FC_D_2:\n"
        "s_cmp_lg_u32 s50, 3\n"
        "s_cbranch_scc1 .Lfb_FC_D_3\n"
        "s_call_b64 s[26:27], .Lfb_F_RED\n"
        "s_call_b64 s[26:27], .Lfb_F_R6\n"
        "s_branch .Lfb_FC_D_x\n"
        ".Lfb_FC_D_3:\n"
        ".Lfb_FC_D_x:\n"
        "s_waitcnt vmcnt(63) lgkmcnt(0)\n"
        "s_mov_b32 s7, 0\n"
        "s_mov_b32 s6, 0\n"
        "s_mov_b64 s[76:77], -1\n"
        "s_mov_b64 exec, -1\n"
        "s_mov_b32 s34, 1114112\n"
        "s_mov_b64 s[72:73], s[62:63]\n"
        "s_mov_b64 s[68:69], s[64:65]\n"
        "s_mov_b64 s[70:71], s[66:67]\n"
        "global_load_ushort v31, v30, s[8:9]\n"
        "global_load_ushort v28, v30, s[8:9] offset:128\n"
        "v_mov_b32 v4, 135568\n"
        "ds_read_b64 v[4:5], v4\n"
        "s_waitcnt lgkmcnt(0)\n"
        "v_readfirstlane_b32 s51, v4\n"
        "v_readfirstlane_b32 s36, v5\n"
        "s_cmp_gt_u32 s51, s36\n"
        "s_cselect_b32 s51, 8192, 0\n"
        "s_add_u32 s36, s30, s51\n"
        "s_addc_u32 s37, s31, 0\n"
        "s_mov_b32 s38, 0x110000\n"
        "s_mov_b32 s39, 0x20000\n"
        "s_xor_b32 s51, s51, 8192\n"
        "s_add_u32 s40, s30, s51\n"
        "s_addc_u32 s41, s31, 0\n"
        "s_mov_b32 s42, 0x110000\n"
        "s_mov_b32 s43, 0x20000\n"
        "v_mov_b32 v8, 135552\n"
        "ds_read_b64 v[8:9], v8\n"
        "s_waitcnt lgkmcnt(0)\n"
        "v_readfirstlane_b32 s56, v8\n"
        "v_readfirstlane_b32 s57, v9\n"
        "s_cmp_lg_u32 s3, 0\n"
        "s_cbranch_scc1 .Lfb_F_6\n"
        "s_sleep 40\n"
        "v_lshlrev_b32 v10, 7, v1\n"
        "v_add_u32 v10, 8192, v10\n"
        "s_mov_b32 s58, 65536\n"
        "buffer_load_dwordx4 v[12:15], v10, s[32:35], 0 offen sc1\n"
        "s_waitcnt vmcnt(0)\n"
        "s_branch .Lfb_F_FL_c\n"
        ".Lfb_F_FL_r:\n"
        "s_mov_b64 exec, vcc\n"
        "s_sleep 4\n"
        "buffer_load_dwordx4 v[12:15], v10, s[32:35], 0 offen sc1\n"
        "s_mov_b64 exec, -1\n"
        "s_sub_u32 s58, s58, 1\n"
        "s_waitcnt vmcnt(0)\n"
        "s_cbranch_scc1 .Lfb_F_FL_d\n"
        ".Lfb_F_FL_c:\n"
        "v_xor_b32 v7, s56, v13\n"
        "v_bitop3_b32 v7, v15, s57, v7 bitop3:0xbe\n"
        "v_cmp_ne_u32 vcc, 0, v7\n"
        "s_cbranch_vccnz .Lfb_F_FL_r\n"
        ".Lfb_F_FL_d:\n"
        ".Lfb_F_6:\n"
        "s_barrier\n"
        "s_mov_b32 s58, 65536\n"
        "buffer_load_dwordx4 v[32:35], v27, s[32:35], 0 offen offset:0 sc1\n"
        "buffer_load_dwordx4 v[36:39], v27, s[32:35], 0 offen offset:256 sc1\n"
        "buffer_load_dwordx4 v[40:43], v27, s[32:35], 0 offen offset:2048 sc1\n"
        "buffer_load_dwordx4 v[44:47], v27, s[32:35], 0 offen offset:2304 sc1\n"
        "buffer_load_dwordx4 v[48:51], v27, s[32:35], s73 offen offset:0 sc1\n"
        "buffer_load_dwordx4 v[52:55], v27, s[32:35], s73 offen offset:256 sc1\n"
        "buffer_load_dwordx4 v[56:59], v20, s[32:35], 0 offen sc1\n"
        "s_waitcnt vmcnt(0)\n"
        "s_branch .Lfb_F_MB_c\n"
        ".Lfb_F_MB_r:\n"
        "s_mov_b64 exec, vcc\n"
        "s_sleep 1\n"
        "buffer_load_dwordx4 v[32:35], v27, s[32:35], 0 offen offset:0 sc1\n"
        "buffer_load_dwordx4 v[36:39], v27, s[32:35], 0 offen offset:256 sc1\n"
        "buffer_load_dwordx4 v[40:43], v27, s[32:35], 0 offen offset:2048 sc1\n"
        "buffer_load_dwordx4 v[44:47], v27, s[32:35], 0 offen offset:2304 sc1\n"
        "buffer_load_dwordx4 v[48:51], v27, s[32:35], s73 offen offset:0 sc1\n"
        "buffer_load_dwordx4 v[52:55], v27, s[32:35], s73 offen offset:256 sc1\n"
        "buffer_load_dwordx4 v[56:59], v20, s[32:35], 0 offen sc1\n"
        "s_mov_b64 exec, -1\n"
        "s_sub_u32 s58, s58, 1\n"
        "s_waitcnt vmcnt(0)\n"
        "s_cbranch_scc1 .Lfb_F_MB_d\n"
        ".Lfb_F_MB_c:\n"
        "v_max3_u32 v7, v32, v33, v34\n"
        "v_max3_u32 v7, v35, v36, v7\n"
        "v_max3_u32 v7, v37, v38, v7\n"
        "v_max3_u32 v7, v39, v40, v7\n"
        "v_max3_u32 v7, v41, v42, v7\n"
        "v_max3_u32 v7, v43, v44, v7\n"
        "v_max3_u32 v7, v45, v46, v7\n"
        "v_max3_u32 v7, v47, v48, v7\n"
        "v_max3_u32 v7, v49, v50, v7\n"
        "v_max3_u32 v7, v51, v52, v7\n"
        "v_max3_u32 v7, v53, v54, v7\n"
        "v_max3_u32 v7, v55, v56, v7\n"
        "v_max3_u32 v7, v57, v58, v7\n"
        "v_max_u32 v7, v59, v7\n"
        "v_cmp_eq_u32 vcc, -1, v7\n"
        "s_cbranch_vccnz .Lfb_F_MB_r\n"
        ".Lfb_F_MB_d:\n"
        "s_mov_b32 s58, 65536\n"
        "buffer_load_dwordx4 v[128:131], v29, s[36:39], 0 offen sc1\n"
        "s_barrier\n"
        ".Lfb_FC_S0:\n"
        "ds_read_b64_tr_b16 v[64:65], v24 offset:0\n"
        "ds_read_b64_tr_b16 v[66:67], v24 offset:256\n"
        "ds_read_b64_tr_b16 v[68:69], v24 offset:2048\n"
        "ds_read_b64_tr_b16 v[70:71], v24 offset:2304\n"
        "ds_read_b64_tr_b16 v[72:73], v24 offset:4096\n"
        "ds_read_b64_tr_b16 v[74:75], v24 offset:4352\n"
        "ds_read_b64_tr_b16 v[76:77], v24 offset:6144\n"
        "ds_read_b64_tr_b16 v[78:79], v24 offset:6400\n"
        "ds_read_b64_tr_b16 v[80:81], v24 offset:8192\n"
        "ds_read_b64_tr_b16 v[82:83], v24 offset:8448\n"
        "ds_read_b64_tr_b16 v[84:85], v24 offset:10240\n"
        "ds_read_b64_tr_b16 v[86:87], v24 offset:10496\n"
        "s_waitcnt lgkmcnt(6)\n"
        "ds_read_b64_tr_b16 v[88:89], v24 offset:12288\n"
        "ds_read_b64_tr_b16 v[90:91], v24 offset:12544\n"
        "ds_read_b64_tr_b16 v[92:93], v24 offset:14336\n"
        "ds_read_b64_tr_b16 v[94:95], v24 offset:14592\n"
        "ds_read_b64_tr_b16 v[96:97], v24 offset:16384\n"
        "ds_read_b64_tr_b16 v[98:99], v24 offset:16640\n"
        "s_waitcnt lgkmcnt(6)\n"
        "ds_read_b64_tr_b16 v[100:101], v24 offset:18432\n"
        "ds_read_b64_tr_b16 v[102:103], v24 offset:18688\n"
        "ds_read_b64_tr_b16 v[104:105], v24 offset:20480\n"
        "ds_read_b64_tr_b16 v[106:107], v24 offset:20736\n"
        "ds_read_b64_tr_b16 v[108:109], v24 offset:22528\n"
        "ds_read_b64_tr_b16 v[110:111], v24 offset:22784\n"
        "s_waitcnt lgkmcnt(6)\n"
        "ds_read_b64_tr_b16 v[112:113], v24 offset:24576\n"
        "ds_read_b64_tr_b16 v[114:115], v24 offset:24832\n"
        "ds_read_b64_tr_b16 v[116:117], v24 offset:26624\n"
        "ds_read_b64_tr_b16 v[118:119], v24 offset:26880\n"
        "ds_read_b64_tr_b16 v[120:121], v24 offset:28672\n"
        "ds_read_b64_tr_b16 v[122:123], v24 offset:28928\n"
        "s_waitcnt lgkmcnt(6)\n"
        "ds_read_b64_tr_b16 v[124:125], v24 offset:30720\n"
        "ds_read_b64_tr_b16 v[126:127], v24 offset:30976\n"
        "v_permlane32_swap_b32 v32, v34\n"
        "v_permlane32_swap_b32 v33, v35\n"
        "v_permlane32_swap_b32 v36, v38\n"
        "v_permlane32_swap_b32 v37, v39\n"
        "v_permlane32_swap_b32 v40, v42\n"
        "v_permlane32_swap_b32 v41, v43\n"
        "v_permlane32_swap_b32 v44, v46\n"
        "v_permlane32_swap_b32 v45, v47\n"
        "v_permlane32_swap_b32 v48, v50\n"
        "v_permlane32_swap_b32 v49, v51\n"
        "v_permlane32_swap_b32 v52, v54\n"
        "v_permlane32_swap_b32 v53, v55\n"
        "v_permlane32_swap_b32 v56, v58\n"
        "v_permlane32_swap_b32 v57, v59\n"
        "v_add_f32 v32, v32, v34\n"
        "v_add_f32 v33, v33, v35\n"
        "v_add_f32 v36, v36, v38\n"
        "v_add_f32 v37, v37, v39\n"
        "v_add_f32 v40, v40, v42\n"
        "v_add_f32 v41, v41, v43\n"
        "v_add_f32 v44, v44, v46\n"
        "v_add_f32 v45, v45, v47\n"
        "v_add_f32 v48, v48, v50\n"
        "v_add_f32 v49, v49, v51\n"
        "v_add_f32 v52, v52, v54\n"
        "v_add_f32 v53, v53, v55\n"
        "v_add_f32 v56, v56, v58\n"
        "v_add_f32 v57, v57, v59\n"
        "v_permlane16_swap_b32 v32, v33\n"
        "v_permlane16_swap_b32 v36, v37\n"
        "v_permlane16_swap_b32 v40, v41\n"
        "v_permlane16_swap_b32 v44, v45\n"
        "v_permlane16_swap_b32 v48, v49\n"
        "v_permlane16_swap_b32 v52, v53\n"
        "v_permlane16_swap_b32 v56, v57\n"
        "v_add_f32 v32, v32, v33\n"
        "v_add_f32 v36, v36, v37\n"
        "v_add_f32 v40, v40, v41\n"
        "v_add_f32 v44, v44, v45\n"
        "v_add_f32 v48, v48, v49\n"
        "v_add_f32 v52, v52, v53\n"
        "v_add_f32 v56, v56, v57\n"
        "v_mov_b32 v33, v36\n"
        "v_mov_b32 v34, v40\n"
        "v_mov_b32 v35, v44\n"
        "v_mov_b32 v36, v48\n"
        "v_mov_b32 v37, v52\n"
        "v_cvt_pk_bf16_f32 v40, v32, v33\n"
        "v_lshlrev_b32 v32, 16, v40\n"
        "v_and_b32 v33, 0xffff0000, v40\n"
        "v_cvt_pk_bf16_f32 v40, v34, v35\n"
        "v_lshlrev_b32 v34, 16, v40\n"
        "v_and_b32 v35, 0xffff0000, v40\n"
        "v_cvt_pk_bf16_f32 v40, v36, v37\n"
        "v_lshlrev_b32 v36, 16, v40\n"
        "v_and_b32 v37, 0xffff0000, v40\n"
        "v_cvt_pk_bf16_f32 v38, v56, v56\n"
        "v_lshlrev_b32 v38, 16, v38\n"
        "v_pk_add_f32 v[36:37], v[36:37], 1.0 op_sel_hi:[1,0]\n"
        "v_cvt_pk_bf16_f32 v40, v36, v37\n"
        "v_lshlrev_b32 v36, 16, v40\n"
        "v_and_b32 v37, 0xffff0000, v40\n"
        "v_lshlrev_b32 v40, 16, v31\n"
        "v_lshlrev_b32 v41, 16, v28\n"
        "s_waitcnt vmcnt(0)\n"
        "s_branch .Lfb_F_F4_c\n"
        ".Lfb_F_F4_r:\n"
        "s_mov_b64 exec, vcc\n"
        "buffer_load_dwordx4 v[128:131], v29, s[36:39], 0 offen sc1\n"
        "s_mov_b64 exec, -1\n"
        "s_sub_u32 s58, s58, 1\n"
        "s_waitcnt vmcnt(0)\n"
        "s_cbranch_scc1 .Lfb_F_F4_d\n"
        ".Lfb_F_F4_c:\n"
        "v_xor_b32 v7, s56, v129\n"
        "v_bitop3_b32 v7, v131, s57, v7 bitop3:0xbe\n"
        "v_cmp_ne_u32 vcc, 0, v7\n"
        "s_cbranch_vccnz .Lfb_F_F4_r\n"
        ".Lfb_F_F4_d:\n"
        "s_cmp_lg_u32 s7, 0\n"
        "s_cbranch_scc1 .Lfb_DRYRET\n"
        ".Lfb_FC_S1:\n"
        "buffer_load_dwordx4 v[192:195], v19, s[36:39], s68 offen offset:0 sc1\n"
        "buffer_load_dwordx4 v[208:211], v19, s[36:39], s68 offen offset:256 sc1\n"
        "buffer_load_dwordx4 v[196:199], v19, s[36:39], s70 offen offset:0 sc1\n"
        "buffer_load_dwordx4 v[212:215], v19, s[36:39], s70 offen offset:256 sc1\n"
        "buffer_load_dwordx4 v[200:203], v19, s[36:39], s69 offen offset:0 sc1\n"
        "buffer_load_dwordx4 v[216:219], v19, s[36:39], s69 offen offset:256 sc1\n"
        "buffer_load_dwordx4 v[204:207], v19, s[36:39], s71 offen offset:0 sc1\n"
        "buffer_load_dwordx4 v[220:223], v19, s[36:39], s71 offen offset:256 sc1\n"
        "s_waitcnt vmcnt(4)\n"
        ".Lfb_F_T3:\n"
        "v_pk_add_f32 v[48:49], v[192:193], v[196:197]\n"
        "v_pk_add_f32 v[50:51], v[194:195], v[198:199]\n"
        "v_pk_add_f32 v[52:53], v[208:209], v[212:213]\n"
        "v_pk_add_f32 v[54:55], v[210:211], v[214:215]\n"
        "s_waitcnt vmcnt(0)\n"
        "v_pk_add_f32 v[128:129], v[200:201], v[204:205]\n"
        "v_pk_add_f32 v[130:131], v[202:203], v[206:207]\n"
        "v_pk_add_f32 v[132:133], v[216:217], v[220:221]\n"
        "v_pk_add_f32 v[134:135], v[218:219], v[222:223]\n"
        "v_pk_add_f32 v[48:49], v[48:49], v[128:129]\n"
        "v_pk_add_f32 v[50:51], v[50:51], v[130:131]\n"
        "v_pk_add_f32 v[52:53], v[52:53], v[132:133]\n"
        "v_pk_add_f32 v[54:55], v[54:55], v[134:135]\n"
        "v_permlane32_swap_b32 v48, v50\n"
        "v_permlane32_swap_b32 v49, v51\n"
        "v_permlane32_swap_b32 v52, v54\n"
        "v_permlane32_swap_b32 v53, v55\n"
        "v_add_f32 v48, v48, v50\n"
        "v_add_f32 v49, v49, v51\n"
        "v_add_f32 v52, v52, v54\n"
        "v_add_f32 v53, v53, v55\n"
        "v_permlane16_swap_b32 v48, v49\n"
        "s_nop 0\n"
        "v_permlane16_swap_b32 v52, v53\n"
        "v_add_f32 v48, v48, v49\n"
        "v_add_f32 v52, v52, v53\n"
        "v_cvt_pk_bf16_f32 v50, v48, v52\n"
        "v_lshlrev_b32 v48, 16, v50\n"
        "v_and_b32 v49, 0xffff0000, v50\n"
        "v_pk_mul_f32 v[48:49], v[48:49], v[32:33]\n"
        "v_cvt_pk_bf16_f32 v50, v48, v49\n"
        "v_lshlrev_b32 v48, 16, v50\n"
        "v_and_b32 v49, 0xffff0000, v50\n"
        "v_pk_add_f32 v[48:49], v[48:49], v[40:41]\n"
        "v_cvt_pk_bf16_f32 v50, v48, v49\n"
        "v_lshlrev_b32 v48, 16, v50\n"
        "v_and_b32 v49, 0xffff0000, v50\n"
        "v_add_f32 v50, v48, v49\n"
        "v_mul_f32 v51, v48, v48\n"
        "v_fmac_f32 v51, v49, v49\n"
        "v_cmp_u_f32 vcc, v50, v50\n"
        "ds_write2st64_b32 v26, v48, v49 offset1:1\n"
        "v_add_f32_dpp v50, v50, v50 quad_perm:[1,0,3,2] row_mask:0xf bank_mask:0xf\n"
        "v_add_f32_dpp v51, v51, v51 quad_perm:[1,0,3,2] row_mask:0xf bank_mask:0xf\n"
        "s_cbranch_vccnz .Lfb_F_R\n"
        ".Lfb_F_C:\n"
        "v_add_f32_dpp v50, v50, v50 quad_perm:[2,3,0,1] row_mask:0xf bank_mask:0xf\n"
        "v_add_f32_dpp v51, v51, v51 quad_perm:[2,3,0,1] row_mask:0xf bank_mask:0xf\n"
        "s_nop 0\n"
        "v_add_f32_dpp v50, v50, v50 row_ror:4 row_mask:0xf bank_mask:0xf\n"
        "v_add_f32_dpp v51, v51, v51 row_ror:4 row_mask:0xf bank_mask:0xf\n"
        "s_nop 0\n"
        "v_add_f32_dpp v50, v50, v50 row_ror:8 row_mask:0xf bank_mask:0xf\n"
        "v_add_f32_dpp v51, v51, v51 row_ror:8 row_mask:0xf bank_mask:0xf\n"
        "s_nop 1\n"
        "v_permlane32_swap_b32 v50, v51\n"
        "v_add_f32 v50, v50, v51\n"
        "v_mov_b32 v51, v50\n"
        "s_nop 1\n"
        "v_permlane16_swap_b32 v50, v51\n"
        "v_add_f32 v50, v50, v51\n"
        "ds_write_b32 v21, v50\n"
        "s_cmp_lg_u32 s7, 0\n"
        "s_cbranch_scc1 .Lfb_DRYRET\n"
        "s_waitcnt lgkmcnt(0)\n"
        "s_barrier\n"
        ".Lfb_FC_S2:\n"
        "ds_read_b128 v[52:55], v22\n"
        "ds_read_b128 v[56:59], v22 offset:16\n"
        "s_waitcnt lgkmcnt(1)\n"
        "v_pk_add_f32 v[52:53], v[52:53], v[54:55]\n"
        "s_waitcnt lgkmcnt(0)\n"
        "v_pk_add_f32 v[56:57], v[56:57], v[58:59]\n"
        "v_pk_add_f32 v[52:53], v[52:53], v[56:57]\n"
        "v_pk_mul_f32 v[52:53], v[52:53], s[72:73] op_sel_hi:[1,0]\n"
        "v_fma_f32 v53, -v52, v52, v53\n"
        "v_cvt_pk_bf16_f32 v52, v52, v52\n"
        "v_max_f32 v53, 0, v53\n"
        "v_lshlrev_b32 v52, 16, v52\n"
        "v_cvt_pk_bf16_f32 v53, v53, v53\n"
        "v_sub_f32 v54, v48, v52\n"
        "v_lshlrev_b32 v53, 16, v53\n"
        "v_sub_f32 v55, v49, v52\n"
        "v_add_f32 v53, 0x3727c5ac, v53\n"
        "v_cvt_pk_bf16_f32 v56, v54, v55\n"
        "v_cvt_pk_bf16_f32 v53, v53, v53\n"
        "v_lshlrev_b32 v54, 16, v56\n"
        "v_lshlrev_b32 v53, 16, v53\n"
        "v_sqrt_f32 v53, v53\n"
        "v_and_b32 v55, 0xffff0000, v56\n"
        "v_cvt_pk_bf16_f32 v53, v53, v53\n"
        "v_lshlrev_b32 v52, 16, v53\n"
        "v_rcp_f32 v56, v52\n"
        "s_nop 0\n"
        "v_pk_mul_f32 v[58:59], v[56:57], v[54:55] op_sel_hi:[0,1]\n"
        "v_pk_fma_f32 v[60:61], v[52:53], v[58:59], v[54:55] op_sel_hi:[0,1,1] neg_lo:[1,0,0] neg_hi:[1,0,0]\n"
        "v_pk_fma_f32 v[58:59], v[56:57], v[60:61], v[58:59] op_sel_hi:[0,1,1]\n"
        "v_cvt_pk_bf16_f32 v60, v58, v59\n"
        "v_lshlrev_b32 v58, 16, v60\n"
        "v_and_b32 v59, 0xffff0000, v60\n"
        "v_pk_mul_f32 v[58:59], v[58:59], v[36:37]\n"
        "v_cvt_pk_bf16_f32 v60, v58, v59\n"
        "v_lshlrev_b32 v58, 16, v60\n"
        "v_and_b32 v59, 0xffff0000, v60\n"
        "v_pk_add_f32 v[58:59], v[58:59], v[34:35]\n"
        "v_cvt_pk_bf16_f32 v58, v58, v59\n"
        "ds_write_b16 v11, v58\n"
        "ds_write_b16_d16_hi v11, v58 offset:128\n"
        "s_cmp_lg_u32 s7, 0\n"
        "s_cbranch_scc1 .Lfb_DRYRET\n"
        "s_waitcnt lgkmcnt(0)\n"
        "s_barrier\n"
        ".Lfb_FC_S3:\n"
        "ds_read_b128 v[144:147], v18 offset:0\n"
        "ds_read_b128 v[148:151], v18 offset:64\n"
        "ds_read_b128 v[152:155], v18 offset:128\n"
        "ds_read_b128 v[156:159], v18 offset:192\n"
        "ds_read_b128 v[160:163], v18 offset:256\n"
        "ds_read_b128 v[164:167], v18 offset:320\n"
        "ds_read_b128 v[168:171], v18 offset:384\n"
        "ds_read_b128 v[172:175], v18 offset:448\n"
        "ds_read_b128 v[176:179], v18 offset:512\n"
        "ds_read_b128 v[180:183], v18 offset:576\n"
        "ds_read_b128 v[184:187], v18 offset:640\n"
        "ds_read_b128 v[188:191], v18 offset:704\n"
        "s_waitcnt lgkmcnt(11)\n"
        "v_mfma_f32_16x16x32_bf16 v[48:51], v[64:67], v[144:147], 0\n"
        "ds_read_b128 v[192:195], v18 offset:768\n"
        "s_waitcnt lgkmcnt(11)\n"
        "v_mfma_f32_16x16x32_bf16 v[48:51], v[68:71], v[148:151], v[48:51]\n"
        "ds_read_b128 v[196:199], v18 offset:832\n"
        "s_waitcnt lgkmcnt(11)\n"
        "v_mfma_f32_16x16x32_bf16 v[48:51], v[72:75], v[152:155], v[48:51]\n"
        "ds_read_b128 v[200:203], v18 offset:896\n"
        "s_waitcnt lgkmcnt(11)\n"
        "v_mfma_f32_16x16x32_bf16 v[48:51], v[76:79], v[156:159], v[48:51]\n"
        "ds_read_b128 v[204:207], v18 offset:960\n"
        "s_waitcnt lgkmcnt(11)\n"
        "v_mfma_f32_16x16x32_bf16 v[48:51], v[80:83], v[160:163], v[48:51]\n"
        "s_waitcnt lgkmcnt(10)\n"
        "v_mfma_f32_16x16x32_bf16 v[48:51], v[84:87], v[164:167], v[48:51]\n"
        "s_waitcnt lgkmcnt(9)\n"
        "v_mfma_f32_16x16x32_bf16 v[48:51], v[88:91], v[168:171], v[48:51]\n"
        "s_waitcnt lgkmcnt(8)\n"
        "v_mfma_f32_16x16x32_bf16 v[48:51], v[92:95], v[172:175], v[48:51]\n"
        "s_waitcnt lgkmcnt(7)\n"
        "v_mfma_f32_16x16x32_bf16 v[48:51], v[96:99], v[176:179], v[48:51]\n"
        "s_waitcnt lgkmcnt(6)\n"
        "v_mfma_f32_16x16x32_bf16 v[48:51], v[100:103], v[180:183], v[48:51]\n"
        "s_waitcnt lgkmcnt(5)\n"
        "v_mfma_f32_16x16x32_bf16 v[48:51], v[104:107], v[184:187], v[48:51]\n"
        "s_waitcnt lgkmcnt(4)\n"
        "v_mfma_f32_16x16x32_bf16 v[48:51], v[108:111], v[188:191], v[48:51]\n"
        "s_waitcnt lgkmcnt(3)\n"
        "v_mfma_f32_16x16x32_bf16 v[48:51], v[112:115], v[192:195], v[48:51]\n"
        "s_waitcnt lgkmcnt(2)\n"
        "v_mfma_f32_16x16x32_bf16 v[48:51], v[116:119], v[196:199], v[48:51]\n"
        "s_waitcnt lgkmcnt(1)\n"
        "v_mfma_f32_16x16x32_bf16 v[48:51], v[120:123], v[200:203], v[48:51]\n"
        "s_waitcnt lgkmcnt(0)\n"
        "v_mfma_f32_16x16x32_bf16 v[48:51], v[124:127], v[204:207], v[48:51]\n"
        "s_lshl_b32 s52, s3, 4\n"
        "s_or_b32 s52, s52, s5\n"
        "ds_read_b32 v44, v16\n"
        "s_nop 4\n"
        "v_permlane32_swap_b32 v48, v50\n"
        "v_permlane32_swap_b32 v49, v51\n"
        "v_cvt_pk_bf16_f32 v52, v48, v49\n"
        "v_cvt_pk_bf16_f32 v53, v50, v51\n"
        "v_lshlrev_b32 v48, 16, v52\n"
        "v_and_b32 v49, 0xffff0000, v52\n"
        "v_pk_mul_f32 v[54:55], v[48:49], v[48:49]\n"
        "v_lshlrev_b32 v50, 16, v53\n"
        "v_pk_fma_f32 v[54:55], v[54:55], s[74:75], v[62:63] op_sel_hi:[1,0,1]\n"
        "v_and_b32 v51, 0xffff0000, v53\n"
        "v_pk_mul_f32 v[54:55], v[54:55], v[48:49]\n"
        "v_exp_f32 v54, v54\n"
        "v_exp_f32 v55, v55\n"
        "s_nop 0\n"
        "v_pk_add_f32 v[54:55], v[54:55], 1.0 op_sel_hi:[1,0]\n"
        "v_rcp_f32 v54, v54\n"
        "v_rcp_f32 v55, v55\n"
        "s_nop 0\n"
        "v_pk_mul_f32 v[48:49], v[48:49], v[54:55]\n"
        "v_cvt_pk_bf16_f32 v52, v48, v49\n"
        "v_lshlrev_b32 v48, 16, v52\n"
        "v_and_b32 v49, 0xffff0000, v52\n"
        "v_pk_mul_f32 v[48:49], v[48:49], v[50:51]\n"
        "v_cvt_pk_bf16_f32 v48, v48, v49\n"
        "ds_write_b32 v23, v48\n"
        "s_cmp_lg_u32 s7, 0\n"
        "s_cbranch_scc1 .Lfb_DRYRET\n"
        "s_waitcnt lgkmcnt(0)\n"
        "s_barrier\n"
        "s_cmp_lt_u32 s52, 16\n"
        "s_cbranch_scc1 .Lfb_F_RED\n"
        "s_endpgm\n"
        ".Lfb_F_RED:\n"
        "s_mov_b32 s6, 0\n"
        "s_mov_b32 s68, 32768\n"
        "s_mov_b32 s69, 65536\n"
        "s_mov_b32 s70, 98304\n"
        "s_mov_b32 s71, 131072\n"
        "s_mov_b32 s72, 163840\n"
        "s_mov_b32 s73, 196608\n"
        "s_sleep 12\n"
        "buffer_load_dwordx4 v[64:67], v25, s[36:39], 0 offen sc1\n"
        "buffer_load_dwordx4 v[68:71], v25, s[36:39], s68 offen sc1\n"
        "buffer_load_dwordx4 v[72:75], v25, s[36:39], s69 offen sc1\n"
        "buffer_load_dwordx4 v[76:79], v25, s[36:39], s70 offen sc1\n"
        "buffer_load_dwordx4 v[80:83], v25, s[36:39], s71 offen sc1\n"
        "buffer_load_dwordx4 v[84:87], v25, s[36:39], s72 offen sc1\n"
        "buffer_load_dwordx4 v[88:91], v25, s[36:39], s73 offen sc1\n"
        "s_waitcnt vmcnt(3)\n"
        ".Lfb_F_T6:\n"
        "v_pk_add_f32 v[96:97], v[64:65], v[68:69]\n"
        "v_pk_add_f32 v[98:99], v[66:67], v[70:71]\n"
        "v_pk_add_f32 v[100:101], v[72:73], v[76:77]\n"
        "v_pk_add_f32 v[102:103], v[74:75], v[78:79]\n"
        "v_pk_add_f32 v[96:97], v[96:97], v[100:101]\n"
        "v_pk_add_f32 v[98:99], v[98:99], v[102:103]\n"
        "s_waitcnt vmcnt(0)\n"
        "v_pk_add_f32 v[100:101], v[80:81], v[84:85]\n"
        "v_pk_add_f32 v[102:103], v[82:83], v[86:87]\n"
        "v_pk_add_f32 v[96:97], v[96:97], v[88:89]\n"
        "v_pk_add_f32 v[98:99], v[98:99], v[90:91]\n"
        "v_pk_add_f32 v[96:97], v[96:97], v[100:101]\n"
        "v_pk_add_f32 v[98:99], v[98:99], v[102:103]\n"
        "s_nop 1\n"
        "v_permlane32_swap_b32 v96, v98\n"
        "v_permlane32_swap_b32 v97, v99\n"
        "v_add_f32 v96, v96, v98\n"
        "v_add_f32 v97, v97, v99\n"
        "s_nop 1\n"
        "v_permlane16_swap_b32 v96, v97\n"
        "v_add_f32 v96, v96, v97\n"
        "s_nop 1\n"
        "v_add_f32_dpp v96, v96, v96 row_ror:8 row_mask:0xf bank_mask:0xf\n"
        "v_cvt_pk_bf16_f32 v97, v96, v96\n"
        "v_cmp_u_f32 vcc, v96, v96\n"
        "v_lshlrev_b32 v97, 16, v97\n"
        "v_mul_f32 v97, v38, v97\n"
        "v_cvt_pk_bf16_f32 v97, v97, v97\n"
        "v_lshlrev_b32 v97, 16, v97\n"
        "v_add_f32 v97, v44, v97\n"
        "v_cvt_pk_bf16_f32 v97, v97, v97\n"
        "s_cbranch_vccnz .Lfb_F_R6\n"
        ".Lfb_F_C6:\n"
        "s_cmp_lg_u32 s7, 0\n"
        "s_cbranch_scc1 .Lfb_DRYRET\n"
        "global_store_short v17, v97, s[28:29]\n"
        "s_cmp_lg_u32 s5, 0\n"
        "s_cbranch_scc1 .Lfb_F_END\n"
        "v_mov_b32 v4, s56\n"
        "v_mov_b32 v5, s57\n"
        "s_mov_b64 exec, 1\n"
        "buffer_store_dwordx2 v[4:5], off, s[32:35], 0 sc1\n"
        ".Lfb_F_END:\n"
        "s_endpgm\n"
        ".Lfb_AL_Q:\n"
        "s_mov_b32 s58, 65536\n"
        "buffer_load_dwordx4 v[32:35], v20, s[36:39], 0 offen sc1\n"
        "buffer_load_dwordx4 v[36:39], v20, s[36:39], s73 offen sc1\n"
        "s_waitcnt vmcnt(0)\n"
        "s_branch .Lfb_AL_QP_c\n"
        ".Lfb_AL_QP_r:\n"
        "s_mov_b64 exec, vcc\n"
        "buffer_load_dwordx4 v[32:35], v20, s[36:39], 0 offen sc1\n"
        "buffer_load_dwordx4 v[36:39], v20, s[36:39], s73 offen sc1\n"
        "s_mov_b64 exec, -1\n"
        "s_sub_u32 s58, s58, 1\n"
        "s_waitcnt vmcnt(0)\n"
        "s_cbranch_scc1 .Lfb_AL_QP_d\n"
        ".Lfb_AL_QP_c:\n"
        "v_add_f32 v44, v32, v36\n"
        "v_add_f32 v45, v34, v38\n"
        "v_xor_b32 v7, s56, v33\n"
        "v_bitop3_b32 v7, v35, s57, v7 bitop3:0xbe\n"
        "v_bitop3_b32 v7, v37, s56, v7 bitop3:0xbe\n"
        "v_bitop3_b32 v7, v39, s57, v7 bitop3:0xbe\n"
        "v_cvt_pk_bf16_f32 v44, v44, v45\n"
        "v_cmp_ne_u32 vcc, 0, v7\n"
        "s_cbranch_vccnz .Lfb_AL_QP_r\n"
        ".Lfb_AL_QP_d:\n"
        "s_branch .Lfb_AL_QOK\n"
        ".Lfb_AL_F:\n"
        "s_mov_b32 s58, 65536\n"
        "buffer_load_dwordx4 v[40:43], v21, s[32:35], 0 offen sc1\n"
        "s_waitcnt vmcnt(0)\n"
        "s_branch .Lfb_AL_FP_c\n"
        ".Lfb_AL_FP_r:\n"
        "s_mov_b64 exec, vcc\n"
        "s_sleep 1\n"
        "buffer_load_dwordx4 v[40:43], v21, s[32:35], 0 offen sc1\n"
        "s_mov_b64 exec, -1\n"
        "s_sub_u32 s58, s58, 1\n"
        "s_waitcnt vmcnt(0)\n"
        "s_cbranch_scc1 .Lfb_AL_FP_d\n"
        ".Lfb_AL_FP_c:\n"
        "v_xor_b32 v7, s56, v41\n"
        "v_bitop3_b32 v7, v43, s57, v7 bitop3:0xbe\n"
        "v_cmp_ne_u32 vcc, 0, v7\n"
        "s_cbranch_vccnz .Lfb_AL_FP_r\n"
        ".Lfb_AL_FP_d:\n"
        "s_branch .Lfb_AL_FOK\n"
        ".Lfb_AC_R:\n"
        "s_cmp_lg_u32 s6, 0\n"
        "s_cbranch_scc1 .Lfb_AC_C\n"
        "s_mov_b32 s6, 1\n"
        "s_mov_b32 s58, 65536\n"
        "buffer_load_dwordx2 v[208:209], v25, s[36:39], 0 offen offset:0 sc1\n"
        "buffer_load_dwordx2 v[210:211], v25, s[36:39], 0 offen offset:512 sc1\n"
        "buffer_load_dwordx2 v[212:213], v25, s[36:39], 0 offen offset:1024 sc1\n"
        "buffer_load_dwordx2 v[214:215], v25, s[36:39], 0 offen offset:1536 sc1\n"
        "buffer_load_dwordx4 v[32:35], v24, s[36:39], 0 offen offset:0 sc1\n"
        "buffer_load_dwordx4 v[36:39], v24, s[36:39], 0 offen offset:2048 sc1\n"
        "buffer_load_dwordx4 v[40:43], v24, s[36:39], s68 offen offset:0 sc1\n"
        "buffer_load_dwordx4 v[44:47], v24, s[36:39], s68 offen offset:2048 sc1\n"
        "s_waitcnt vmcnt(0)\n"
        "s_branch .Lfb_AC_R_p_c\n"
        ".Lfb_AC_R_p_r:\n"
        "s_mov_b64 exec, vcc\n"
        "buffer_load_dwordx2 v[208:209], v25, s[36:39], 0 offen offset:0 sc1\n"
        "buffer_load_dwordx2 v[210:211], v25, s[36:39], 0 offen offset:512 sc1\n"
        "buffer_load_dwordx2 v[212:213], v25, s[36:39], 0 offen offset:1024 sc1\n"
        "buffer_load_dwordx2 v[214:215], v25, s[36:39], 0 offen offset:1536 sc1\n"
        "buffer_load_dwordx4 v[32:35], v24, s[36:39], 0 offen offset:0 sc1\n"
        "buffer_load_dwordx4 v[36:39], v24, s[36:39], 0 offen offset:2048 sc1\n"
        "buffer_load_dwordx4 v[40:43], v24, s[36:39], s68 offen offset:0 sc1\n"
        "buffer_load_dwordx4 v[44:47], v24, s[36:39], s68 offen offset:2048 sc1\n"
        "s_mov_b64 exec, -1\n"
        "s_sub_u32 s58, s58, 1\n"
        "s_waitcnt vmcnt(0)\n"
        "s_cbranch_scc1 .Lfb_AC_R_p_d\n"
        ".Lfb_AC_R_p_c:\n"
        "v_max3_u32 v7, v32, v33, v34\n"
        "v_max3_u32 v7, v35, v36, v7\n"
        "v_max3_u32 v7, v37, v38, v7\n"
        "v_max3_u32 v7, v39, v40, v7\n"
        "v_max3_u32 v7, v41, v42, v7\n"
        "v_max3_u32 v7, v43, v44, v7\n"
        "v_max3_u32 v7, v45, v46, v7\n"
        "v_max3_u32 v7, v47, v208, v7\n"
        "v_max3_u32 v7, v209, v210, v7\n"
        "v_max3_u32 v7, v211, v212, v7\n"
        "v_max3_u32 v7, v213, v214, v7\n"
        "v_max_u32 v7, v215, v7\n"
        "v_cmp_eq_u32 vcc, -1, v7\n"
        "s_cbranch_vccnz .Lfb_AC_R_p_r\n"
        ".Lfb_AC_R_p_d:\n"
        "s_branch .Lfb_AC_T2\n"
        ".Lfb_F_R:\n"
        "s_cmp_lg_u32 s6, 0\n"
        "s_cbranch_scc1 .Lfb_F_C\n"
        "s_mov_b32 s6, 1\n"
        "s_mov_b32 s58, 65536\n"
        "buffer_load_dwordx4 v[192:195], v19, s[36:39], s68 offen offset:0 sc1\n"
        "buffer_load_dwordx4 v[208:211], v19, s[36:39], s68 offen offset:256 sc1\n"
        "buffer_load_dwordx4 v[196:199], v19, s[36:39], s70 offen offset:0 sc1\n"
        "buffer_load_dwordx4 v[212:215], v19, s[36:39], s70 offen offset:256 sc1\n"
        "buffer_load_dwordx4 v[200:203], v19, s[36:39], s69 offen offset:0 sc1\n"
        "buffer_load_dwordx4 v[216:219], v19, s[36:39], s69 offen offset:256 sc1\n"
        "buffer_load_dwordx4 v[204:207], v19, s[36:39], s71 offen offset:0 sc1\n"
        "buffer_load_dwordx4 v[220:223], v19, s[36:39], s71 offen offset:256 sc1\n"
        "s_waitcnt vmcnt(0)\n"
        "s_branch .Lfb_F_R_p_c\n"
        ".Lfb_F_R_p_r:\n"
        "s_mov_b64 exec, vcc\n"
        "buffer_load_dwordx4 v[192:195], v19, s[36:39], s68 offen offset:0 sc1\n"
        "buffer_load_dwordx4 v[208:211], v19, s[36:39], s68 offen offset:256 sc1\n"
        "buffer_load_dwordx4 v[196:199], v19, s[36:39], s70 offen offset:0 sc1\n"
        "buffer_load_dwordx4 v[212:215], v19, s[36:39], s70 offen offset:256 sc1\n"
        "buffer_load_dwordx4 v[200:203], v19, s[36:39], s69 offen offset:0 sc1\n"
        "buffer_load_dwordx4 v[216:219], v19, s[36:39], s69 offen offset:256 sc1\n"
        "buffer_load_dwordx4 v[204:207], v19, s[36:39], s71 offen offset:0 sc1\n"
        "buffer_load_dwordx4 v[220:223], v19, s[36:39], s71 offen offset:256 sc1\n"
        "s_mov_b64 exec, -1\n"
        "s_sub_u32 s58, s58, 1\n"
        "s_waitcnt vmcnt(0)\n"
        "s_cbranch_scc1 .Lfb_F_R_p_d\n"
        ".Lfb_F_R_p_c:\n"
        "v_max3_u32 v7, v192, v193, v194\n"
        "v_max3_u32 v7, v195, v196, v7\n"
        "v_max3_u32 v7, v197, v198, v7\n"
        "v_max3_u32 v7, v199, v200, v7\n"
        "v_max3_u32 v7, v201, v202, v7\n"
        "v_max3_u32 v7, v203, v204, v7\n"
        "v_max3_u32 v7, v205, v206, v7\n"
        "v_max3_u32 v7, v207, v208, v7\n"
        "v_max3_u32 v7, v209, v210, v7\n"
        "v_max3_u32 v7, v211, v212, v7\n"
        "v_max3_u32 v7, v213, v214, v7\n"
        "v_max3_u32 v7, v215, v216, v7\n"
        "v_max3_u32 v7, v217, v218, v7\n"
        "v_max3_u32 v7, v219, v220, v7\n"
        "v_max3_u32 v7, v221, v222, v7\n"
        "v_max_u32 v7, v223, v7\n"
        "v_cmp_eq_u32 vcc, -1, v7\n"
        "s_cbranch_vccnz .Lfb_F_R_p_r\n"
        ".Lfb_F_R_p_d:\n"
        "s_branch .Lfb_F_T3\n"
        ".Lfb_F_R6:\n"
        "s_cmp_lg_u32 s6, 0\n"
        "s_cbranch_scc1 .Lfb_F_C6\n"
        "s_mov_b32 s6, 1\n"
        "s_mov_b32 s58, 65536\n"
        "buffer_load_dwordx4 v[64:67], v25, s[36:39], 0 offen sc1\n"
        "buffer_load_dwordx4 v[68:71], v25, s[36:39], s68 offen sc1\n"
        "buffer_load_dwordx4 v[72:75], v25, s[36:39], s69 offen sc1\n"
        "buffer_load_dwordx4 v[76:79], v25, s[36:39], s70 offen sc1\n"
        "buffer_load_dwordx4 v[80:83], v25, s[36:39], s71 offen sc1\n"
        "buffer_load_dwordx4 v[84:87], v25, s[36:39], s72 offen sc1\n"
        "buffer_load_dwordx4 v[88:91], v25, s[36:39], s73 offen sc1\n"
        "s_waitcnt vmcnt(0)\n"
        "s_branch .Lfb_F_R6_p_c\n"
        ".Lfb_F_R6_p_r:\n"
        "s_mov_b64 exec, vcc\n"
        "buffer_load_dwordx4 v[64:67], v25, s[36:39], 0 offen sc1\n"
        "buffer_load_dwordx4 v[68:71], v25, s[36:39], s68 offen sc1\n"
        "buffer_load_dwordx4 v[72:75], v25, s[36:39], s69 offen sc1\n"
        "buffer_load_dwordx4 v[76:79], v25, s[36:39], s70 offen sc1\n"
        "buffer_load_dwordx4 v[80:83], v25, s[36:39], s71 offen sc1\n"
        "buffer_load_dwordx4 v[84:87], v25, s[36:39], s72 offen sc1\n"
        "buffer_load_dwordx4 v[88:91], v25, s[36:39], s73 offen sc1\n"
        "s_mov_b64 exec, -1\n"
        "s_sub_u32 s58, s58, 1\n"
        "s_waitcnt vmcnt(0)\n"
        "s_cbranch_scc1 .Lfb_F_R6_p_d\n"
        ".Lfb_F_R6_p_c:\n"
        "v_max3_u32 v7, v64, v65, v66\n"
        "v_max3_u32 v7, v67, v68, v7\n"
        "v_max3_u32 v7, v69, v70, v7\n"
        "v_max3_u32 v7, v71, v72, v7\n"
        "v_max3_u32 v7, v73, v74, v7\n"
        "v_max3_u32 v7, v75, v76, v7\n"
        "v_max3_u32 v7, v77, v78, v7\n"
        "v_max3_u32 v7, v79, v80, v7\n"
        "v_max3_u32 v7, v81, v82, v7\n"
        "v_max3_u32 v7, v83, v84, v7\n"
        "v_max3_u32 v7, v85, v86, v7\n"
        "v_max3_u32 v7, v87, v88, v7\n"
        "v_max3_u32 v7, v89, v90, v7\n"
        "v_max_u32 v7, v91, v7\n"
        "v_cmp_eq_u32 vcc, -1, v7\n"
        "s_cbranch_vccnz .Lfb_F_R6_p_r\n"
        ".Lfb_F_R6_p_d:\n"
        "s_branch .Lfb_F_T6\n"
        ".Lfb_DRYRET:\n"
        "s_setpc_b64 s[26:27]\n"
        ".Lfb_KEND:\n"
        :
        : "s"(__builtin_amdgcn_kernarg_segment_ptr()), "s"(blockIdx.x), "v"(threadIdx.x)
        : "v1", "v2", "v3", "v4", "v5", "v6", "v7", "v8", "v9", "v10", "v11", "v12", "v13", "v14", "v15", "v16", "v17", "v18", "v19", "v20", "v21", "v22", "v23", "v24", "v25", "v26", "v27", "v28", "v29", "v30", "v31", "v32", "v33", "v34", "v35", "v36", "v37", "v38", "v39", "v40", "v41", "v42", "v43", "v44", "v45", "v46", "v47", "v48", "v49", "v50", "v51", "v52", "v53", "v54", "v55", "v56", "v57", "v58", "v59", "v60", "v61", "v62", "v63", "v64", "v65", "v66", "v67", "v68", "v69", "v70", "v71", "v72", "v73", "v74", "v75", "v76", "v77", "v78", "v79", "v80", "v81", "v82", "v83", "v84", "v85", "v86", "v87", "v88", "v89", "v90", "v91", "v92", "v93", "v94", "v95", "v96", "v97", "v98", "v99", "v100", "v101", "v102", "v103", "v104", "v105", "v106", "v107", "v108", "v109", "v110", "v111", "v112", "v113", "v114", "v115", "v116", "v117", "v118", "v119", "v120", "v121", "v122", "v123", "v124", "v125", "v126", "v127", "v128", "v129", "v130", "v131", "v132", "v133", "v134", "v135", "v136", "v137", "v138", "v139", "v140", "v141", "v142", "v143", "v144", "v145", "v146", "v147", "v148", "v149", "v150", "v151", "v152", "v153", "v154", "v155", "v156", "v157", "v158", "v159", "v160", "v161", "v162", "v163", "v164", "v165", "v166", "v167", "v168", "v169", "v170", "v171", "v172", "v173", "v174", "v175", "v176", "v177", "v178", "v179", "v180", "v181", "v182", "v183", "v184", "v185", "v186", "v187", "v188", "v189", "v190", "v191", "v192", "v193", "v194", "v195", "v196", "v197", "v198", "v199", "v200", "v201", "v202", "v203", "v204", "v205", "v206", "v207", "v208", "v209", "v210", "v211", "v212", "v213", "v214", "v215", "v216", "v217", "v218", "v219", "v220", "v221", "v222", "v223", "v224", "v225", "v226", "v227", "v228", "v229", "v230", "v231", "v232", "v233", "v234", "v235", "v236", "v237", "v238", "v239", "s3", "s4", "s5", "s6", "s7", "s8", "s9", "s10", "s11", "s12", "s13", "s14", "s15", "s16", "s17", "s18", "s19", "s20", "s21", "s22", "s23", "s24", "s25", "s26", "s27", "s28", "s29", "s30", "s31", "s33", "s34", "s35", "s36", "s37", "s38", "s39", "s40", "s41", "s42", "s43", "s44", "s45", "s46", "s47", "s48", "s49", "s50", "s51", "s52", "s53", "s54", "s55", "s56", "s57", "s58", "s59", "s60", "s61", "s62", "s63", "s64", "s65", "s66", "s67", "s68", "s69", "s70", "s71", "s72", "s73", "s74", "s75", "s76", "s77", "vcc", "scc", "memory");
}

extern "C" void kernel_launch(void* const* d_in, const int* in_sizes, int n_in, void* d_out, int out_size, void* d_ws, size_t ws_size,
                              hipStream_t stream) {
    Params p;
    p.x = (const u16*)d_in[0]; p.kc = (const u16*)d_in[1]; p.vc = (const u16*)d_in[2]; p.wq = (const u16*)d_in[3]; p.wo = (const u16*)d_in[4];
    p.wg = (const u16*)d_in[6]; p.wu = (const u16*)d_in[7]; p.wd = (const u16*)d_in[8]; p.c = (const u16*)d_in[9]; p.wm = (const u16*)d_in[10];
    p.out = (u16*)d_out; p.ws = (unsigned char*)d_ws + WS_OFF(d_ws, ws_size);
    (void)hipFuncSetAttribute((const void*)fused_block, hipFuncAttributeMaxDynamicSharedMemorySize, LDS_BYTES);
    hipLaunchKernelGGL(fused_block, dim3(256), dim3(512), LDS_BYTES, stream, p);
}
```
